```python
import torch

def _softmax(x, axis=-1):
    return torch.softmax(x, dim=axis)

def _gelu(x):
    return torch.nn.functional.gelu(x, approximate='tanh')

def _silu(x):
    return torch.nn.functional.silu(x)

def _sigmoid(x):
    return torch.sigmoid(x)

def _layernorm(x, axis=-1, eps=1e-05):
    m = x.mean(dim=axis, keepdim=True)
    v = x.var(dim=axis, keepdim=True, unbiased=False)
    return (x - m) / torch.sqrt(v + eps)

def _scatter_add(tgt, idx, src, axis=0):
    return tgt.clone().index_add_(axis, idx, src)

def _segment_sum(src, seg, n):
    out = torch.zeros((n,) + src.shape[1:], dtype=src.dtype)
    return out.index_add_(0, seg, src)

def _pad(x, pw):
    flat = tuple((v for pair in reversed(pw) for v in pair))
    return torch.nn.functional.pad(x, flat)
kernel_name = 'interleave_layers__sliding_local_dilated__mixer_gmlp__prompt'

def setup_inputs():

    _w = lambda a: (lambda t: t.to(torch.bfloat16) if t.is_floating_point() else t)(torch.as_tensor(a))

    def _A_setup():
        B, S, D = (1, 2048, 512)
        return {'x': _w((lambda *s: torch.randn(*s, dtype=torch.bfloat16) * 0.05)(B, S, D)), 'wqkv': _w((lambda *s: torch.randn(*s, dtype=torch.bfloat16) * 0.1)(D, 3 * D)), 'wo': _w((lambda *s: torch.randn(*s, dtype=torch.bfloat16) * 0.1)(D, D))}
    _A_in = _A_setup()

    def _B_setup():
        B, S, D, F = (1, 2048, 512, 1376)
        Fh = F // 2
        return {'x': _w((lambda *s: torch.randn(*s, dtype=torch.bfloat16) * 0.05)(B, S, D)), 'w_in': _w((lambda *s: torch.randn(*s, dtype=torch.bfloat16) * 0.4)(D, 2 * Fh)), 'w_sp': _w(0.01 * (lambda *s: torch.randn(*s, dtype=torch.bfloat16) * 0.4)(S, S)), 'b_sp': _w(1.0 + 0.01 * (lambda *s: torch.randn(*s, dtype=torch.bfloat16) * 0.05)(S)), 'w_out': _w((lambda *s: torch.randn(*s, dtype=torch.bfloat16) * 0.05)(Fh, D))}
    _B_in = _B_setup()
    return {'A_x': _A_in['x'], 'A_wqkv': _A_in['wqkv'], 'A_wo': _A_in['wo'], 'B_x': _B_in['x'], 'B_w_in': _B_in['w_in'], 'B_w_sp': _B_in['w_sp'], 'B_b_sp': _B_in['b_sp'], 'B_w_out': _B_in['w_out']}

def reference(A_x, A_wqkv, A_wo, B_x, B_w_in, B_w_sp, B_b_sp, B_w_out):

    def _A_ref(x, wqkv, wo):
        B, S, D = x.shape
        H = 32
        Dh = D // H
        W, dil = (8, 2)
        qkv = (lambda x, *d: x.permute(*d))((x @ wqkv).reshape(B, S, 3, H, Dh), 2, 0, 3, 1, 4)
        q, k, v = (qkv[0], qkv[1], qkv[2])
        scale = Dh ** (-0.5)
        pos = torch.arange(S)
        off = torch.arange(W) * dil
        idx = pos[:, None] - off[None, :]
        mask = idx >= 0
        idx_c = torch.where(mask, idx, 0).reshape(-1)
        kg = k[:, :, idx_c, :].reshape(B, H, S, W, Dh)
        vg = v[:, :, idx_c, :].reshape(B, H, S, W, Dh)
        logits = torch.einsum('bhsd,bhswd->bhsw', q, kg) * scale
        logits = torch.where(mask[None, None, :, :], logits, -1000000000.0)
        attn = _softmax(logits, axis=-1)
        o = torch.einsum('bhsw,bhswd->bhsd', attn, vg)
        return (lambda x, *d: x.permute(*d))(o, 0, 2, 1, 3).reshape(B, S, D) @ wo

    def _B_ref(x, w_in, w_sp, b_sp, w_out):
        B, S, D = x.shape
        F = w_in.shape[-1]
        Fh = F // 2
        z = _gelu(torch.einsum('bsd,df->bsf', x, w_in))
        z1 = z[..., :Fh].clone()
        z2 = _layernorm(z[..., Fh:], axis=-1)
        g = torch.einsum('ts,bsf->btf', w_sp, z2) + b_sp[None, :, None]
        return torch.einsum('bsf,fd->bsd', z1 * g, w_out)
    x = A_x
    x = x + _A_ref(_layernorm(x, axis=-1), A_wqkv, A_wo)
    x = x + _B_ref(_layernorm(x, axis=-1), B_w_in, B_w_sp, B_b_sp, B_w_out)
    x = x + _A_ref(_layernorm(x, axis=-1), A_wqkv, A_wo)
    x = x + _B_ref(_layernorm(x, axis=-1), B_w_in, B_w_sp, B_b_sp, B_w_out)
    return x
```

```cpp
#include <hip/hip_runtime.h>
#include <hip/hip_bf16.h>
#include <stdint.h>

typedef __attribute__((ext_vector_type(8))) short bf16x8;
typedef __attribute__((ext_vector_type(4))) short s16x4;
typedef __attribute__((ext_vector_type(4))) float f32x4;
typedef __attribute__((ext_vector_type(2))) __bf16 bf16v2;
typedef __attribute__((ext_vector_type(4))) unsigned u32x4;
typedef __attribute__((ext_vector_type(2))) unsigned u32x2;
typedef unsigned short u16;
typedef unsigned int u32;
typedef unsigned long long u64;

#define LDS_AS __attribute__((address_space(3)))
#define GLB_AS __attribute__((address_space(1)))
#define DEVI __device__ __forceinline__

constexpr int S_ = 2048, D_ = 512, FH = 688, FHP = 704, F2 = 1376;

DEVI float bf2f(u32 v) { return __uint_as_float(v << 16); }
DEVI float bflo(u32 w) { return __uint_as_float(w << 16); }
DEVI float bfhi(u32 w) { return __uint_as_float(w & 0xffff0000u); }
DEVI u32 pack2(float a, float b) {
  bf16v2 v; v[0] = (__bf16)a; v[1] = (__bf16)b;
  return __builtin_bit_cast(u32, v);
}
DEVI float dot2(u32 a, u32 b, float c) {
  return __builtin_amdgcn_fdot2_f32_bf16(__builtin_bit_cast(bf16v2, a), __builtin_bit_cast(bf16v2, b), c, false);
}
DEVI float rbf(float f) { return bf2f((u32)__builtin_bit_cast(u16, (__bf16)f)); }

DEVI void glds16(const void* sbase, u32 voff, u32 lds) {
  u32 keep;
  asm volatile("s_mov_b32 %0, m0\n\ts_mov_b32 m0, %3\n\ts_nop 0\n\tglobal_load_lds_dwordx4 %1, %2\n\ts_mov_b32 m0, %0"
               : "=&s"(keep) : "v"(voff), "s"(sbase), "s"(lds) : "memory");
}
DEVI u32 lds_addr(const void* p) { return (u32)(size_t)(LDS_AS const char*)p; }
template <int N> DEVI void wait_vm() { asm volatile("s_waitcnt vmcnt(%0)" ::"n"(N) : "memory"); }
DEVI void wait_lgkm0() { asm volatile("s_waitcnt lgkmcnt(0)" ::: "memory"); }
DEVI void bar() { wait_lgkm0(); __builtin_amdgcn_s_barrier(); }

template <int CTRL> DEVI float dpp_f(float x) {
  return __builtin_bit_cast(float, __builtin_amdgcn_update_dpp(0, __builtin_bit_cast(int, x), CTRL, 0xF, 0xF, true));
}
DEVI float row16_sum(float x) {
  x += dpp_f<0xB1>(x);
  x += dpp_f<0x4E>(x);
  x += dpp_f<0x141>(x);
  x += dpp_f<0x140>(x);
  return x;
}

DEVI bf16x8 lds_b128(const char* p) { return *(const bf16x8*)p; }
DEVI bf16x8 lds_tr2(const char* p) {
  s16x4 a = __builtin_amdgcn_ds_read_tr16_b64_v4i16((LDS_AS s16x4*)p);
  s16x4 b = __builtin_amdgcn_ds_read_tr16_b64_v4i16((LDS_AS s16x4*)(p + 512));
  return __builtin_shufflevector(a, b, 0, 1, 2, 3, 4, 5, 6, 7);
}
DEVI f32x4 mfma(bf16x8 a, bf16x8 b, f32x4 c) { return __builtin_amdgcn_mfma_f32_16x16x32_bf16(a, b, c, 0, 0, 0); }

template <int I> struct IC { static constexpr int v = I; };
template <int N, int I = 0, class F> DEVI void sfor(F&& f) {
  if constexpr (I < N) { f(IC<I>{}); sfor<N, I + 1>(f); }
}

DEVI float gelu_t(float x) {
  float u = 0.7978845608028654f * (x + 0.044715f * x * x * x);
  float e = __expf(-2.0f * u);
  return x * __builtin_amdgcn_rcpf(1.0f + e);
}

template <int NG> DEVI void ln_rows_lds(char* sA, int row0, int lane) {
  const int sub = lane >> 4, j = lane & 15;
#pragma unroll
  for (int g = 0; g < NG; ++g) {
    char* rp = sA + (row0 + 4 * g + sub) * 1024 + j * 16;
    u32x4 v[4];
#pragma unroll
    for (int i = 0; i < 4; ++i) v[i] = *(const u32x4*)(rp + i * 256);
    float s = 0.f;
#pragma unroll
    for (int i = 0; i < 4; ++i)
#pragma unroll
      for (int e = 0; e < 4; ++e) s += bflo(v[i][e]) + bfhi(v[i][e]);
    s = row16_sum(s);
    const float mean = s * (1.0f / 512.0f);
    float q = 0.f;
#pragma unroll
    for (int i = 0; i < 4; ++i)
#pragma unroll
      for (int e = 0; e < 4; ++e) {
        float a = bflo(v[i][e]) - mean, b = bfhi(v[i][e]) - mean;
        q += a * a + b * b;
      }
    q = row16_sum(q);
    const float mb = rbf(mean);
    const float vb = rbf(q * (1.0f / 512.0f));
    const float sd = rbf(__builtin_sqrtf(rbf(vb + 1e-5f)));
    const float rs = 1.0f / sd;
#pragma unroll
    for (int i = 0; i < 4; ++i) {
      u32x4 o;
#pragma unroll
      for (int e = 0; e < 4; ++e) {
        float a = rbf(bflo(v[i][e]) - mb) * rs, b = rbf(bfhi(v[i][e]) - mb) * rs;
        o[e] = pack2(a, b);
      }
      *(u32x4*)(rp + i * 256) = o;
    }
  }
}

constexpr int K1_ABYTES = 80 * 1024;
constexpr int K1_BST = 64 * 192 * 2;
constexpr int K1_NST = 3;

__global__ __launch_bounds__(256, 1) void k1_qkv_attn(const u16* __restrict__ x, const u16* __restrict__ w,
                                                      u16* __restrict__ o, u32* __restrict__ zero_area,
                                                      int zero_words) {
  __shared__ __attribute__((aligned(16))) char smem[K1_ABYTES + K1_NST * K1_BST];
  char* sA = smem;
  char* sB = smem + K1_ABYTES;
  const int tid = threadIdx.x, lane = tid & 63;
  const int wid = __builtin_amdgcn_readfirstlane(tid >> 6);
  const int bid = blockIdx.x, xcd = bid & 7, idx = bid >> 3;
  const int rt = (xcd >> 1) * 8 + (idx & 7), hg = (xcd & 1) * 4 + (idx >> 3);
  const int r0 = rt * 64;
  const u32 lb = lds_addr(smem);

#pragma unroll
  for (int i = 0; i < 20; ++i) {
    int er = wid * 20 + i;
    int gr = r0 - 16 + er; gr = gr < 0 ? 0 : gr;
    glds16(x + (size_t)gr * D_, (u32)((lane ^ (er & 15)) * 16), lb + er * 1024);
  }
  auto loadB = [&](int kt, int buf) {
#pragma unroll
    for (int i = 0; i < 6; ++i) {
      int pi = wid * 6 + i, panel = pi >> 3, p = pi & 7;
      int f = ((lane >> 4) & 1) | ((p & 1) << 1);
      int sc = ((((lane & 7) >> 1) ^ f) << 1) | (lane & 1);
      glds16(w + (size_t)(kt * 64 + 8 * p) * 1536 + panel * 512 + hg * 64, (u32)((lane >> 3) * 3072 + sc * 16),
             lb + K1_ABYTES + buf * K1_BST + panel * 8192 + p * 1024);
    }
  };
  loadB(0, 0);
  loadB(1, 1);
  for (int i = bid * 256 + tid; i < zero_words; i += 256 * 256) zero_area[i] = 0u;

  wait_vm<12>();
  ln_rows_lds<5>(sA, wid * 20, lane);

  f32x4 acc[14];
#pragma unroll
  for (int i = 0; i < 14; ++i) acc[i] = (f32x4){0.f, 0.f, 0.f, 0.f};
  const int l15 = lane & 15, g4 = lane >> 4;
  int aoff[4];
#pragma unroll
  for (int v = 0; v < 4; ++v) aoff[v] = l15 * 1024 + (((4 * v + g4) ^ l15) << 4);
  const int fl = ((lane >> 3) & 1) | (((lane >> 4) & 1) << 1);
  const int boff = (8 * g4 + (l15 >> 2)) * 128 + 8 * (lane & 3) + ((wid ^ fl) << 5);

  sfor<8>([&](auto ktc) {
    constexpr int kt = decltype(ktc)::v;
    if constexpr (kt < 7) wait_vm<6>(); else wait_vm<0>();
    bar();
    if constexpr (kt + 2 < 8) loadB(kt + 2, (kt + 2) % 3);
    const char* bs = sB + (kt % 3) * K1_BST + boff;
#pragma unroll
    for (int ks = 0; ks < 2; ++ks) {
      bf16x8 af[5], bf[3];
#pragma unroll
      for (int mt = 0; mt < 5; ++mt) af[mt] = lds_b128(sA + mt * 16384 + (kt >> 1) * 256 + aoff[(kt & 1) * 2 + ks]);
#pragma unroll
      for (int pn = 0; pn < 3; ++pn) bf[pn] = lds_tr2(bs + pn * 8192 + ks * 4096);
#pragma unroll
      for (int mt = 1; mt < 5; ++mt) acc[mt - 1] = mfma(bf[0], af[mt], acc[mt - 1]);
#pragma unroll
      for (int mt = 0; mt < 5; ++mt) acc[4 + mt] = mfma(bf[1], af[mt], acc[4 + mt]);
#pragma unroll
      for (int mt = 0; mt < 5; ++mt) acc[9 + mt] = mfma(bf[2], af[mt], acc[9 + mt]);
    }
  });
  bar();
  char* sw = sB + wid * 7168;
  char* sq = sw; char* sk = sw + 2048; char* sv = sw + 2048 + 2560;
  {
    const int half = g4 >> 1, o8 = (g4 & 1) * 8;
#pragma unroll
    for (int mt = 0; mt < 4; ++mt) {
      u32x2 pq = {pack2(acc[mt][0], acc[mt][1]), pack2(acc[mt][2], acc[mt][3])};
      *(u32x2*)(sq + half * 1024 + (mt * 16 + l15) * 16 + o8) = pq;
    }
#pragma unroll
    for (int mt = 0; mt < 5; ++mt) {
      u32x2 pk = {pack2(acc[4 + mt][0], acc[4 + mt][1]), pack2(acc[4 + mt][2], acc[4 + mt][3])};
      u32x2 pv = {pack2(acc[9 + mt][0], acc[9 + mt][1]), pack2(acc[9 + mt][2], acc[9 + mt][3])};
      *(u32x2*)(sk + half * 1280 + (mt * 16 + l15) * 16 + o8) = pk;
      *(u32x2*)(sv + half * 1280 + (mt * 16 + l15) * 16 + o8) = pv;
    }
  }
  wait_lgkm0();
  {
    const int t = r0 + lane;
    u32x4 q0 = *(const u32x4*)(sq + lane * 16), q1 = *(const u32x4*)(sq + 1024 + lane * 16);
    float lg[8];
    float mx = -3.0e38f;
#pragma unroll
    for (int j = 0; j < 8; ++j) {
      int er = lane + 16 - 2 * j;
      u32x4 k0 = *(const u32x4*)(sk + er * 16), k1 = *(const u32x4*)(sk + 1280 + er * 16);
      float d = 0.f;
#pragma unroll
      for (int e = 0; e < 4; ++e)
        d = dot2(q0[e], k0[e], d);
#pragma unroll
      for (int e = 0; e < 4; ++e)
        d = dot2(q1[e], k1[e], d);
      d = rbf(rbf(d) * 0.25f);
      d = (t - 2 * j >= 0) ? d : -1.0e9f;
      lg[j] = d;
      mx = fmaxf(mx, d);
    }
    float sum = 0.f;
#pragma unroll
    for (int j = 0; j < 8; ++j) { lg[j] = __expf(lg[j] - mx); sum += lg[j]; }
    const float rs = 1.0f / sum;
    float ov[16];
#pragma unroll
    for (int e = 0; e < 16; ++e) ov[e] = 0.f;
#pragma unroll
    for (int jp = 0; jp < 4; ++jp) {
      const u32 pp = pack2(lg[2 * jp] * rs, lg[2 * jp + 1] * rs);
      int ea = lane + 16 - 4 * jp, eb = ea - 2;
      u32x4 va0 = *(const u32x4*)(sv + ea * 16), va1 = *(const u32x4*)(sv + 1280 + ea * 16);
      u32x4 vb0 = *(const u32x4*)(sv + eb * 16), vb1 = *(const u32x4*)(sv + 1280 + eb * 16);
#pragma unroll
      for (int e = 0; e < 4; ++e) {
        u32 lo = __builtin_amdgcn_perm(vb0[e], va0[e], 0x05040100u);
        u32 hi = __builtin_amdgcn_perm(vb0[e], va0[e], 0x07060302u);
        ov[2 * e] = dot2(lo, pp, ov[2 * e]);
        ov[2 * e + 1] = dot2(hi, pp, ov[2 * e + 1]);
        u32 lo1 = __builtin_amdgcn_perm(vb1[e], va1[e], 0x05040100u);
        u32 hi1 = __builtin_amdgcn_perm(vb1[e], va1[e], 0x07060302u);
        ov[8 + 2 * e] = dot2(lo1, pp, ov[8 + 2 * e]);
        ov[8 + 2 * e + 1] = dot2(hi1, pp, ov[8 + 2 * e + 1]);
      }
    }
    u32x4 w0, w1;
#pragma unroll
    for (int e = 0; e < 4; ++e) { w0[e] = pack2(ov[2 * e], ov[2 * e + 1]); w1[e] = pack2(ov[8 + 2 * e], ov[8 + 2 * e + 1]); }
    u16* op = o + (size_t)t * D_ + hg * 64 + wid * 16;
    *(u32x4*)op = w0;
    *(u32x4*)(op + 8) = w1;
  }
}

template <int LDA, int NT, int KW, int NST>
__global__ __launch_bounds__(256, 1) void k_proj_res(const u16* __restrict__ A, const u16* __restrict__ W,
                                                     const u16* __restrict__ res, u16* __restrict__ out) {
  constexpr int ST = 16384;
  __shared__ __attribute__((aligned(16))) char smem[NST * ST];
  const int tid = threadIdx.x, lane = tid & 63;
  const int wid = __builtin_amdgcn_readfirstlane(tid >> 6);
  const int bid = blockIdx.x, xcd = bid & 7, idx = bid >> 3;
  const int mt0 = (xcd >> 1) * 8 + (idx & 7), nt0 = (xcd & 1) * 4 + (idx >> 3);
  const int r0 = mt0 * 64, c0 = nt0 * 64;
  const int l15 = lane & 15, g4 = lane >> 4;
  const int wm = wid >> 1, wn = wid & 1;
  const u32 lb = lds_addr(smem);

  u32x2 rv[2][2];
#pragma unroll
  for (int i = 0; i < 2; ++i)
#pragma unroll
    for (int j = 0; j < 2; ++j)
      rv[i][j] = *(const u32x2*)(res + (size_t)(r0 + wm * 32 + i * 16 + l15) * D_ + c0 + wn * 32 + j * 16 + 4 * g4);

  auto load = [&](int kt, int buf) {
#pragma unroll
    for (int i = 0; i < 2; ++i) {
      int p = wid * 2 + i;
      int sc = (lane & 7) ^ (lane >> 3);
      glds16(A + (size_t)(r0 + 8 * p) * LDA + kt * 64, (u32)((lane >> 3) * (LDA * 2) + sc * 16), lb + buf * ST + p * 1024);
    }
#pragma unroll
    for (int i = 0; i < 2; ++i) {
      int p = wid * 2 + i;
      int f = ((lane >> 4) & 1) | ((p & 1) << 1);
      int sc = ((((lane & 7) >> 1) ^ f) << 1) | (lane & 1);
      int krow = kt * 64 + 8 * p + (lane >> 3);
      krow = krow < KW ? krow : KW - 1;
      glds16(W + c0, (u32)(krow * (D_ * 2) + sc * 16), lb + buf * ST + 8192 + p * 1024);
    }
  };
  sfor<NST - 1>([&](auto i) { load(decltype(i)::v, decltype(i)::v); });

  f32x4 acc[2][2];
#pragma unroll
  for (int i = 0; i < 2; ++i)
#pragma unroll
    for (int j = 0; j < 2; ++j) acc[i][j] = (f32x4){0.f, 0.f, 0.f, 0.f};
  int aoff[2], boff[2];
  const int fl = ((lane >> 3) & 1) | (((lane >> 4) & 1) << 1);
#pragma unroll
  for (int ks = 0; ks < 2; ++ks) aoff[ks] = (wm * 32 + l15) * 128 + (((4 * ks + g4) ^ (l15 & 7)) << 4);
#pragma unroll
  for (int j = 0; j < 2; ++j) boff[j] = 8192 + (8 * g4 + (l15 >> 2)) * 128 + 8 * (lane & 3) + (((wn * 2 + j) ^ fl) << 5);

  sfor<NT>([&](auto ktc) {
    constexpr int kt = decltype(ktc)::v;
    constexpr int rem = NT - 1 - kt;
    wait_vm<4 * (rem < NST - 2 ? rem : NST - 2)>();
    bar();
    if constexpr (kt + NST - 1 < NT) load(kt + NST - 1, (kt + NST - 1) % NST);
    const char* sb = smem + (kt % NST) * ST;
#pragma unroll
    for (int ks = 0; ks < 2; ++ks) {
      bf16x8 af[2], bf[2];
#pragma unroll
      for (int i = 0; i < 2; ++i) af[i] = lds_b128(sb + i * 2048 + aoff[ks]);
#pragma unroll
      for (int j = 0; j < 2; ++j) bf[j] = lds_tr2(sb + boff[j] + ks * 4096);
#pragma unroll
      for (int i = 0; i < 2; ++i)
#pragma unroll
        for (int j = 0; j < 2; ++j) acc[i][j] = mfma(bf[j], af[i], acc[i][j]);
    }
  });
#pragma unroll
  for (int i = 0; i < 2; ++i)
#pragma unroll
    for (int j = 0; j < 2; ++j) {
      u32x2 r = rv[i][j], ov;
      ov[0] = pack2(bflo(r[0]) + rbf(acc[i][j][0]), bfhi(r[0]) + rbf(acc[i][j][1]));
      ov[1] = pack2(bflo(r[1]) + rbf(acc[i][j][2]), bfhi(r[1]) + rbf(acc[i][j][3]));
      *(u32x2*)(out + (size_t)(r0 + wm * 32 + i * 16 + l15) * D_ + c0 + wn * 32 + j * 16 + 4 * g4) = ov;
    }
}

constexpr int K3_ABYTES = 64 * 1024;
constexpr int K3_BST = 64 * 192 * 2;
constexpr int K3_NST = 3;

template <bool Z2>
DEVI void k3_body(char* smem, const u16* __restrict__ w, u16* __restrict__ z1, u16* __restrict__ z2t,
                  u64* __restrict__ gran, int rt, int ct, int lane, int wid, int tid) {
  char* sA = smem;
  char* sB = smem + K3_ABYTES;
  const int r0 = rt * 64;
  const int cbase = (Z2 ? FH : 0) + ct * 192;
  const u32 lb = lds_addr(smem);
  auto loadB = [&](int kt, int buf) {
#pragma unroll
    for (int i = 0; i < 6; ++i) {
      int pi = wid * 6 + i, panel = pi >> 3, p = pi & 7;
      int f = ((lane >> 4) & 1) | ((p & 1) << 1);
      int sc = ((((lane & 7) >> 1) ^ f) << 1) | (lane & 1);
      int col = cbase + panel * 64 + sc * 8;
      col = col <= F2 - 8 ? col : F2 - 8;
      glds16(w + (size_t)(kt * 64 + 8 * p) * F2, (u32)((lane >> 3) * (F2 * 2) + col * 2),
             lb + K3_ABYTES + buf * K3_BST + panel * 8192 + p * 1024);
    }
  };
  loadB(0, 0);
  loadB(1, 1);
  wait_vm<12>();
  ln_rows_lds<4>(sA, wid * 16, lane);

  f32x4 acc[4][3];
#pragma unroll
  for (int i = 0; i < 4; ++i)
#pragma unroll
    for (int j = 0; j < 3; ++j) acc[i][j] = (f32x4){0.f, 0.f, 0.f, 0.f};
  const int l15 = lane & 15, g4 = lane >> 4;
  int aoff[4], boff[3];
#pragma unroll
  for (int v = 0; v < 4; ++v) aoff[v] = l15 * 1024 + (((4 * v + g4) ^ l15) << 4);
  const int fl = ((lane >> 3) & 1) | (((lane >> 4) & 1) << 1);
#pragma unroll
  for (int j = 0; j < 3; ++j) {
    int nt = wid * 3 + j;
    boff[j] = (nt >> 2) * 8192 + (8 * g4 + (l15 >> 2)) * 128 + 8 * (lane & 3) + (((nt & 3) ^ fl) << 5);
  }
  sfor<8>([&](auto ktc) {
    constexpr int kt = decltype(ktc)::v;
    if constexpr (kt < 7) wait_vm<6>(); else wait_vm<0>();
    bar();
    if constexpr (kt + 2 < 8) loadB(kt + 2, (kt + 2) % 3);
    const char* bs = sB + (kt % 3) * K3_BST;
#pragma unroll
    for (int ks = 0; ks < 2; ++ks) {
      bf16x8 af[4], bf[3];
#pragma unroll
      for (int mt = 0; mt < 4; ++mt) af[mt] = lds_b128(sA + mt * 16384 + (kt >> 1) * 256 + aoff[(kt & 1) * 2 + ks]);
#pragma unroll
      for (int j = 0; j < 3; ++j) bf[j] = lds_tr2(bs + boff[j] + ks * 4096);
#pragma unroll
      for (int mt = 0; mt < 4; ++mt)
#pragma unroll
        for (int j = 0; j < 3; ++j) {
          if constexpr (Z2) acc[mt][j] = mfma(af[mt], bf[j], acc[mt][j]);
          else acc[mt][j] = mfma(bf[j], af[mt], acc[mt][j]);
        }
    }
  });
  const int u0 = ct * 12 + wid * 3;
  int nv = 43 - u0; nv = nv < 0 ? 0 : (nv > 3 ? 3 : nv);
#pragma unroll
  for (int mt = 0; mt < 4; ++mt)
#pragma unroll
    for (int j = 0; j < 3; ++j)
#pragma unroll
      for (int e = 0; e < 4; ++e) acc[mt][j][e] = rbf(gelu_t(rbf(acc[mt][j][e])));
  if constexpr (!Z2) {
#pragma unroll
    for (int mt = 0; mt < 4; ++mt)
#pragma unroll
      for (int j = 0; j < 3; ++j)
        if (j < nv) {
          u32x2 ov = {pack2(acc[mt][j][0], acc[mt][j][1]), pack2(acc[mt][j][2], acc[mt][j][3])};
          *(u32x2*)(z1 + (size_t)(r0 + mt * 16 + l15) * FH + (u0 + j) * 16 + 4 * g4) = ov;
        }
  } else {
    bar();
    float* st = (float*)smem;
    float* fin = (float*)(smem + 4096);
    const float inv_n = nv > 0 ? 1.0f / (16.0f * nv) : 0.f;
#pragma unroll
    for (int mt = 0; mt < 4; ++mt)
#pragma unroll
      for (int e = 0; e < 4; ++e) {
        float s = 0.f;
#pragma unroll
        for (int j = 0; j < 3; ++j) s += (j < nv) ? acc[mt][j][e] : 0.f;
        s = row16_sum(s);
        float mw = s * inv_n;
        float q = 0.f;
#pragma unroll
        for (int j = 0; j < 3; ++j) { float d = acc[mt][j][e] - mw; q += (j < nv) ? d * d : 0.f; }
        q = row16_sum(q);
        if (l15 == 0) {
          int row = mt * 16 + 4 * g4 + e;
          st[(wid * 64 + row) * 2] = mw;
          st[(wid * 64 + row) * 2 + 1] = q;
        }
      }
    bar();
    if (tid < 64) {
      float n = 0.f, mean = 0.f, m2 = 0.f;
#pragma unroll
      for (int ww = 0; ww < 4; ++ww) {
        int nw = 43 - (ct * 12 + ww * 3); nw = nw < 0 ? 0 : (nw > 3 ? 3 : nw);
        float nb = 16.0f * nw;
        if (nw > 0) {
          float mb = st[(ww * 64 + tid) * 2], qb = st[(ww * 64 + tid) * 2 + 1];
          float nn = n + nb, d = mb - mean;
          mean += d * (nb / nn);
          m2 += qb + d * d * (n * nb / nn);
          n = nn;
        }
      }
      u64* gbase = gran + (size_t)(rt * 4) * 128;
      __hip_atomic_store(gbase + ct * 128 + tid * 2, (1ull << 32) | (u64)__float_as_uint(mean), __ATOMIC_RELAXED, __HIP_MEMORY_SCOPE_AGENT);
      __hip_atomic_store(gbase + ct * 128 + tid * 2 + 1, (1ull << 32) | (u64)__float_as_uint(m2), __ATOMIC_RELAXED, __HIP_MEMORY_SCOPE_AGENT);
      float tn = 0.f, tmean = 0.f, tm2 = 0.f;
#pragma unroll
      for (int c = 0; c < 4; ++c) {
        float mb = mean, qb = m2;
        if (c != ct) {
          u64 a, b;
          int spins = 0;
          do {
            a = __hip_atomic_load(gbase + c * 128 + tid * 2, __ATOMIC_RELAXED, __HIP_MEMORY_SCOPE_AGENT);
            b = __hip_atomic_load(gbase + c * 128 + tid * 2 + 1, __ATOMIC_RELAXED, __HIP_MEMORY_SCOPE_AGENT);
            if ((a >> 32) == 1ull && (b >> 32) == 1ull) break;
            __builtin_amdgcn_s_sleep(1);
          } while (++spins < (1 << 22));
          mb = __uint_as_float((u32)a); qb = __uint_as_float((u32)b);
        }
        float nb = c < 3 ? 192.0f : 112.0f;
        float nn = tn + nb, d = mb - tmean;
        tmean += d * (nb / nn);
        tm2 += qb + d * d * (tn * nb / nn);
        tn = nn;
      }
      const float mbf = rbf(tmean);
      const float vb = rbf(tm2 * (1.0f / 688.0f));
      const float sd = rbf(__builtin_sqrtf(rbf(vb + 1e-5f)));
      fin[tid * 2] = mbf;
      fin[tid * 2 + 1] = 1.0f / sd;
    }
    bar();
#pragma unroll
    for (int mt = 0; mt < 4; ++mt) {
      f32x4 m01 = *(const f32x4*)(fin + (mt * 16 + 4 * g4) * 2), m23 = *(const f32x4*)(fin + (mt * 16 + 4 * g4) * 2 + 4);
#pragma unroll
      for (int j = 0; j < 3; ++j)
        if (j < nv) {
          u32x2 ov;
          ov[0] = pack2(rbf(acc[mt][j][0] - m01[0]) * m01[1], rbf(acc[mt][j][1] - m01[2]) * m01[3]);
          ov[1] = pack2(rbf(acc[mt][j][2] - m23[0]) * m23[1], rbf(acc[mt][j][3] - m23[2]) * m23[3]);
          *(u32x2*)(z2t + (size_t)((u0 + j) * 16 + l15) * S_ + r0 + mt * 16 + 4 * g4) = ov;
        }
    }
  }
}

__global__ __launch_bounds__(256, 1) void k3_in_gelu(const u16* __restrict__ x, const u16* __restrict__ w,
                                                     u16* __restrict__ z1, u16* __restrict__ z2t,
                                                     u64* __restrict__ gran) {
  __shared__ __attribute__((aligned(16))) char smem[K3_ABYTES + K3_NST * K3_BST];
  const int tid = threadIdx.x, lane = tid & 63;
  const int wid = __builtin_amdgcn_readfirstlane(tid >> 6);
  const int bid = blockIdx.x, xcd = bid & 7, idx = bid >> 3;
  const int rt = (xcd >> 1) * 8 + (idx & 7), c8 = (xcd & 1) * 4 + (idx >> 3);
  const int r0 = rt * 64;
#pragma unroll
  for (int i = 0; i < 16; ++i) {
    int er = wid * 16 + i;
    glds16(x + (size_t)(r0 + er) * D_, (u32)((lane ^ (er & 15)) * 16), lds_addr(smem) + er * 1024);
  }
  if (c8 < 4) k3_body<false>(smem, w, z1, z2t, gran, rt, c8, lane, wid, tid);
  else k3_body<true>(smem, w, z1, z2t, gran, rt, c8 - 4, lane, wid, tid);
}

constexpr int K4_ST = (64 + 96) * 128;
constexpr int K4_NST = 7;
constexpr int K4_NT = 32;

__global__ __launch_bounds__(256, 1) void k4_spatial(const u16* __restrict__ wsp, const u16* __restrict__ z2t,
                                                     const u16* __restrict__ z1, const u16* __restrict__ bsp,
                                                     u16* __restrict__ h) {
  __shared__ __attribute__((aligned(16))) char smem[K4_NST * K4_ST];
  const int tid = threadIdx.x, lane = tid & 63;
  const int wid = __builtin_amdgcn_readfirstlane(tid >> 6);
  const int bid = blockIdx.x, xcd = bid & 7, idx = bid >> 3;
  const int mt0 = (xcd >> 1) * 8 + (idx & 7), nt0 = (xcd & 1) * 4 + (idx >> 3);
  const int r0 = mt0 * 64;
  const int u0 = nt0 * 6 < 37 ? nt0 * 6 : 37;
  const int f0 = u0 * 16;
  const int l15 = lane & 15, g4 = lane >> 4;
  const int wm = wid >> 1, wn = wid & 1;

  u32x2 zv[2][3];
  float bs[2];
#pragma unroll
  for (int i = 0; i < 2; ++i) {
    bs[i] = bf2f(bsp[r0 + wm * 32 + i * 16 + l15]);
#pragma unroll
    for (int j = 0; j < 3; ++j)
      zv[i][j] = *(const u32x2*)(z1 + (size_t)(r0 + wm * 32 + i * 16 + l15) * FH + f0 + wn * 48 + j * 16 + 4 * g4);
  }
  const u16* src[5];
#pragma unroll
  for (int i = 0; i < 5; ++i) {
    int pi = wid * 5 + i;
    src[i] = (pi < 8 ? wsp + (size_t)(r0 + 8 * pi) * S_ : z2t + (size_t)(f0 + 8 * (pi - 8)) * S_);
  }
  const u32 lb = lds_addr(smem);
  const u32 voff = (u32)((lane >> 3) * (S_ * 2) + (((lane & 7) ^ (lane >> 3)) << 4));
  auto load = [&](int kt, int buf) {
#pragma unroll
    for (int i = 0; i < 5; ++i) glds16(src[i] + kt * 64, voff, lb + buf * K4_ST + (wid * 5 + i) * 1024);
  };
  sfor<K4_NST - 1>([&](auto i) { load(decltype(i)::v, decltype(i)::v); });

  f32x4 acc[2][3];
#pragma unroll
  for (int i = 0; i < 2; ++i)
#pragma unroll
    for (int j = 0; j < 3; ++j) acc[i][j] = (f32x4){0.f, 0.f, 0.f, 0.f};
  int aoff[2], boff[2];
#pragma unroll
  for (int ks = 0; ks < 2; ++ks) {
    aoff[ks] = (wm * 32 + l15) * 128 + (((4 * ks + g4) ^ (l15 & 7)) << 4);
    boff[ks] = 8192 + (wn * 48 + l15) * 128 + (((4 * ks + g4) ^ (l15 & 7)) << 4);
  }
  auto comp = [&](int buf) {
    const char* sb = smem + buf * K4_ST;
#pragma unroll
    for (int ks = 0; ks < 2; ++ks) {
      bf16x8 af[2], bf[3];
#pragma unroll
      for (int i = 0; i < 2; ++i) af[i] = lds_b128(sb + i * 2048 + aoff[ks]);
#pragma unroll
      for (int j = 0; j < 3; ++j) bf[j] = lds_b128(sb + j * 2048 + boff[ks]);
#pragma unroll
      for (int i = 0; i < 2; ++i)
#pragma unroll
        for (int j = 0; j < 3; ++j) acc[i][j] = mfma(bf[j], af[i], acc[i][j]);
    }
  };
  int rd = 0, wr = K4_NST - 1;
  for (int kt = 0; kt < K4_NT - (K4_NST - 1); ++kt) {
    wait_vm<5 * (K4_NST - 2)>();
    bar();
    load(kt + K4_NST - 1, wr);
    comp(rd);
    rd = rd + 1 == K4_NST ? 0 : rd + 1;
    wr = wr + 1 == K4_NST ? 0 : wr + 1;
  }
  sfor<K4_NST - 1>([&](auto ic) {
    constexpr int i = decltype(ic)::v;
    wait_vm<5 * (K4_NST - 2 - i)>();
    bar();
    comp(rd);
    rd = rd + 1 == K4_NST ? 0 : rd + 1;
  });
  const int umin = nt0 * 6;
#pragma unroll
  for (int i = 0; i < 2; ++i) {
    u16* hp = h + (size_t)(r0 + wm * 32 + i * 16 + l15) * FHP;
#pragma unroll
    for (int j = 0; j < 3; ++j) {
      int u = u0 + wn * 3 + j;
      if (u >= umin) {
        u32x2 z = zv[i][j], ov;
        float g0 = rbf(rbf(acc[i][j][0]) + bs[i]), g1 = rbf(rbf(acc[i][j][1]) + bs[i]);
        float g2 = rbf(rbf(acc[i][j][2]) + bs[i]), g3 = rbf(rbf(acc[i][j][3]) + bs[i]);
        ov[0] = pack2(bflo(z[0]) * g0, bfhi(z[0]) * g1);
        ov[1] = pack2(bflo(z[1]) * g2, bfhi(z[1]) * g3);
        *(u32x2*)(hp + u * 16 + 4 * g4) = ov;
      }
    }
    if (nt0 == 7 && wn == 1) *(u32x2*)(hp + FH + 4 * g4) = (u32x2){0u, 0u};
  }
}

extern "C" void kernel_launch(void* const* d_in, const int* in_sizes, int n_in, void* d_out, int out_size,
                              void* d_ws, size_t ws_size, hipStream_t stream) {
  const u16* x = (const u16*)d_in[0];
  const u16* wqkv = (const u16*)d_in[1];
  const u16* wo = (const u16*)d_in[2];
  const u16* w_in = (const u16*)d_in[4];
  const u16* w_sp = (const u16*)d_in[5];
  const u16* b_sp = (const u16*)d_in[6];
  const u16* w_out = (const u16*)d_in[7];
  u16* out = (u16*)d_out;
  char* ws = (char*)d_ws;
  constexpr size_t MB = 1 << 20;
  u64* gran = (u64*)ws;
  u16* o = (u16*)(ws + 1 * MB);
  u16* x1 = (u16*)(ws + 4 * MB);
  u16* x2 = (u16*)(ws + 7 * MB);
  u16* z1 = (u16*)(ws + 10 * MB);
  u16* z2t = (u16*)(ws + 14 * MB);
  u16* h = (u16*)(ws + 18 * MB);
  const int gran_words = 2 * 32 * 4 * 128 * 2;

  for (int L = 0; L < 2; ++L) {
    const u16* xin = L == 0 ? x : x2;
    u16* xout = L == 0 ? x2 : out;
    k1_qkv_attn<<<256, 256, 0, stream>>>(xin, wqkv, o, (u32*)gran, L == 0 ? gran_words : 0);
    k_proj_res<512, 8, 512, 8><<<256, 256, 0, stream>>>(o, wo, xin, x1);
    k3_in_gelu<<<256, 256, 0, stream>>>(x1, w_in, z1, z2t, gran + (size_t)L * 32 * 4 * 128);
    k4_spatial<<<256, 256, 0, stream>>>(w_sp, z2t, z1, b_sp, h);
    k_proj_res<FHP, 11, FH, 8><<<256, 256, 0, stream>>>(h, w_out, x1, xout);
  }
}
```

```cpp
#include <hip/hip_runtime.h>
#include <hip/hip_bf16.h>
#include <stdint.h>

typedef __attribute__((ext_vector_type(8))) short bf16x8;
typedef __attribute__((ext_vector_type(4))) short s16x4;
typedef __attribute__((ext_vector_type(4))) float f32x4;
typedef __attribute__((ext_vector_type(2))) float f32x2;
typedef __attribute__((ext_vector_type(2))) __bf16 bf16v2;
typedef __attribute__((ext_vector_type(4))) unsigned u32x4;
typedef __attribute__((ext_vector_type(2))) unsigned u32x2;
typedef unsigned short u16;
typedef unsigned int u32;
typedef unsigned long long u64;

#define LDS_AS __attribute__((address_space(3)))
#define GLB_AS __attribute__((address_space(1)))
#define DEVI __device__ __forceinline__

constexpr int S_ = 2048, D_ = 512, FH = 688, FHP = 704, F2 = 1376;

DEVI float bf2f(u32 v) { return __uint_as_float(v << 16); }
DEVI float bflo(u32 w) { return __uint_as_float(w << 16); }
DEVI float bfhi(u32 w) { return __uint_as_float(w & 0xffff0000u); }
DEVI u32 pack2(float a, float b) {
  bf16v2 v; v[0] = (__bf16)a; v[1] = (__bf16)b;
  return __builtin_bit_cast(u32, v);
}
DEVI float dot2(u32 a, u32 b, float c) {
  return __builtin_amdgcn_fdot2_f32_bf16(__builtin_bit_cast(bf16v2, a), __builtin_bit_cast(bf16v2, b), c, false);
}
DEVI float rbf(float f) { return bf2f((u32)__builtin_bit_cast(u16, (__bf16)f)); }

DEVI void glds16(const void* sbase, u32 voff, u32 lds) {
  u32 keep;
  asm volatile("s_mov_b32 %0, m0\n\ts_mov_b32 m0, %3\n\ts_nop 0\n\tglobal_load_lds_dwordx4 %1, %2\n\ts_mov_b32 m0, %0"
               : "=&s"(keep) : "v"(voff), "s"(sbase), "s"(lds) : "memory");
}
DEVI void touch(const void* sbase, u32 voff, u32 lds) {
  u32 keep;
  asm volatile("s_mov_b32 %0, m0\n\ts_mov_b32 m0, %3\n\ts_nop 0\n\tglobal_load_lds_dword %1, %2\n\ts_mov_b32 m0, %0"
               : "=&s"(keep) : "v"(voff), "s"(sbase), "s"(lds) : "memory");
}
DEVI u32 lds_addr(const void* p) { return (u32)(size_t)(LDS_AS const char*)p; }
template <int N> DEVI void wait_vm() { asm volatile("s_waitcnt vmcnt(%0)" ::"n"(N) : "memory"); }
#define GSTAMP(k, i)
DEVI void wait_lgkm0() {
  __builtin_amdgcn_sched_barrier(0);
  __builtin_amdgcn_s_waitcnt(0xC07F);
  asm volatile("" ::: "memory");
  __builtin_amdgcn_sched_barrier(0);
}
DEVI void bar() { wait_lgkm0(); __builtin_amdgcn_s_barrier(); __builtin_amdgcn_sched_barrier(0); }

template <int NRD, int NMM> DEVI void interleave() {
#pragma unroll
  for (int i = 0; i < NRD; ++i) {
    __builtin_amdgcn_sched_group_barrier(0x008, 1, 0);
    __builtin_amdgcn_sched_group_barrier(0x100, 1, 0);
  }
  __builtin_amdgcn_sched_group_barrier(0x008, NMM - NRD, 0);
}
template <int NT, int NST, int NRD, int NMM, class Rd, class Mm>
DEVI void ring_consumer(Rd&& rdfr, Mm&& mm, int kid = 0) {
  bar();
  GSTAMP(kid, 8);
  rdfr(0, 0);
  int rd = 0;
#pragma unroll 1
  for (int kt = 0; kt < NT - 1; ++kt) {
    wait_lgkm0();
    rdfr(1, rd);
    mm(0);
    interleave<NRD, NMM>();
    rd = rd + 1 == NST ? 0 : rd + 1;
    bar();
    GSTAMP(kid, 9 + kt);
    rdfr(0, rd);
    mm(1);
    interleave<NRD, NMM>();
  }
  wait_lgkm0();
  rdfr(1, rd);
  mm(0);
  interleave<NRD, NMM>();
  wait_lgkm0();
  mm(1);
}

template <int CTRL> DEVI float dpp_f(float x) {
  return __builtin_bit_cast(float, __builtin_amdgcn_update_dpp(0, __builtin_bit_cast(int, x), CTRL, 0xF, 0xF, true));
}
DEVI float row16_sum(float x) {
  x += dpp_f<0xB1>(x);
  x += dpp_f<0x4E>(x);
  x += dpp_f<0x141>(x);
  x += dpp_f<0x140>(x);
  return x;
}

DEVI bf16x8 lds_b128(const char* p) { return *(const bf16x8*)p; }
DEVI bf16x8 lds_tr2(const char* p) {
  s16x4 a = __builtin_amdgcn_ds_read_tr16_b64_v4i16((LDS_AS s16x4*)p);
  s16x4 b = __builtin_amdgcn_ds_read_tr16_b64_v4i16((LDS_AS s16x4*)(p + 512));
  return __builtin_shufflevector(a, b, 0, 1, 2, 3, 4, 5, 6, 7);
}
DEVI f32x4 mfma(bf16x8 a, bf16x8 b, f32x4 c) { return __builtin_amdgcn_mfma_f32_16x16x32_bf16(a, b, c, 0, 0, 0); }

#define STAMP_DECL
#define STAMP(i)
#define STAMP_OUT(k, n)

template <int I> struct IC { static constexpr int v = I; };
template <int N, int I = 0, class F> DEVI void sfor(F&& f) {
  if constexpr (I < N) { f(IC<I>{}); sfor<N, I + 1>(f); }
}

DEVI f32x2 gelu2(f32x2 x) {
  const f32x2 c1 = {-2.3022081985f, -2.3022081985f};
  const f32x2 c2 = {-0.10294324f, -0.10294324f};
  const f32x2 one = {1.0f, 1.0f};
  f32x2 a = x * (x * x * c2 + c1);
  f32x2 e = {__builtin_amdgcn_exp2f(a[0]), __builtin_amdgcn_exp2f(a[1])};
  f32x2 d = e + one;
  f32x2 r = {__builtin_amdgcn_rcpf(d[0]), __builtin_amdgcn_rcpf(d[1])};
  return x * r;
}
DEVI f32x2 unpk(u32 w) { return (f32x2){bflo(w), bfhi(w)}; }

template <int NT, int NST, int LPW, int PRE, class Load> DEVI void ring_loader(Load&& load) {
  static_assert(PRE + 1 <= NST && PRE + 1 <= NT, "ring");
#pragma unroll 1
  for (int i = 0; i < PRE; ++i) load(i, i);
  wait_vm<LPW * (PRE - 1)>();
  bar();
  load(PRE, PRE);
  int wr = PRE + 1 == NST ? 0 : PRE + 1;
#pragma unroll 1
  for (int kt = 0; kt < NT - 1 - PRE; ++kt) {
    wait_vm<LPW * (PRE - 1)>();
    bar();
    load(kt + PRE + 1, wr);
    wr = wr + 1 == NST ? 0 : wr + 1;
  }
  sfor<PRE>([&](auto ic) {
    constexpr int i = decltype(ic)::v;
    wait_vm<LPW * (PRE - 1 - i)>();
    bar();
  });
}

DEVI f32x2 stat_fin(const f32x4& sm, const f32x4& sg, int lane) {
  const int j = lane & 3;
  float dg = j == 0 ? sg[0] : (j == 1 ? sg[1] : (j == 2 ? sg[2] : sg[3]));
  const float q = __shfl(dg, (lane & 15) + 16 * ((lane & 15) >> 2));
  const float mean = sm[0] * (1.0f / 512.0f);
  const float var = fmaxf(q * (1.0f / 512.0f) - mean * mean, 0.f);
  const float sd = rbf(__builtin_sqrtf(rbf(rbf(var) + 1e-5f)));
  return (f32x2){rbf(mean), 1.0f / sd};
}

constexpr int K1_AST = 80 * 128;
constexpr int K1_ST = K1_AST + 64 * 192 * 2;
constexpr int K1_NST = 4;

__global__ __launch_bounds__(512, 2) void k1_qkv_attn(const u16* __restrict__ x, const u16* __restrict__ w,
                                                      u16* __restrict__ o, u32* __restrict__ zero_area,
                                                      int zero_words) {
  __shared__ __attribute__((aligned(16))) char smem[K1_NST * K1_ST + 1024];
  f32x2* sst = (f32x2*)(smem + K1_NST * K1_ST);
  const int tid = threadIdx.x, lane = tid & 63;
  const int wid = __builtin_amdgcn_readfirstlane(tid >> 6);
  const int bid = blockIdx.x, xcd = bid & 7, idx = bid >> 3;
  const int rt = (xcd >> 1) * 8 + (idx & 7), hg = (xcd & 1) * 4 + (idx >> 3);
  const int r0 = rt * 64;
  const u32 lb = lds_addr(smem);
  const int hw = wid & 3, mh = wid >> 2;
  const int l15 = lane & 15, g4 = lane >> 4;
  constexpr int QH = 1152, KH = 1408, HSZ = 2 * QH + 4 * KH;
  STAMP_DECL STAMP(0);
  if (wid >= 4) {
    const int lw = wid - 4;
    u32 bvoff[2];
#pragma unroll
    for (int i = 0; i < 2; ++i) {
      int f = ((lane >> 4) & 1) | (i << 1);
      int sc = ((((lane & 7) >> 1) ^ f) << 1) | (lane & 1);
      bvoff[i] = (u32)((lane >> 3) * 3072 + sc * 16);
    }
    const int ap0 = lw < 2 ? lw * 3 : 6 + (lw - 2) * 2;
    u32 avoff[3];
#pragma unroll
    for (int i = 0; i < 3; ++i) {
      int gr = r0 - 16 + 8 * (ap0 + i) + (lane >> 3); gr = gr < 0 ? 0 : (gr > S_ - 1 ? S_ - 1 : gr);
      avoff[i] = (u32)(gr * (D_ * 2) + (((lane & 7) ^ (lane >> 3)) << 4));
    }
    auto load = [&](int kt, int buf, int na) {
#pragma unroll
      for (int i = 0; i < 3; ++i)
        if (i < na) glds16(x + kt * 64, avoff[i], lb + buf * K1_ST + (ap0 + i) * 1024);
#pragma unroll
      for (int i = 0; i < 6; ++i) {
        int pi = lw * 6 + i, panel = pi >> 3, p = pi & 7;
        glds16(w + (size_t)(kt * 64 + 8 * p) * 1536 + panel * 512 + hg * 64, bvoff[i & 1],
               lb + buf * K1_ST + K1_AST + panel * 8192 + p * 1024);
      }
    };
    if (lw < 2) ring_loader<8, K1_NST, 9, 2>([&](int kt, int buf) { load(kt, buf, 3); });
    else ring_loader<8, K1_NST, 8, 2>([&](int kt, int buf) { load(kt, buf, 2); });
    for (int i = bid * 256 + (tid - 256); i < zero_words; i += 256 * 256) zero_area[i] = 0u;
    bar();
  } else {
    f32x4 acc[14], cs[3];
#pragma unroll
    for (int i = 0; i < 14; ++i) acc[i] = (f32x4){0.f, 0.f, 0.f, 0.f};
#pragma unroll
    for (int i = 0; i < 3; ++i) cs[i] = (f32x4){0.f, 0.f, 0.f, 0.f};
    const bf16x8 ones = {0x3F80, 0x3F80, 0x3F80, 0x3F80, 0x3F80, 0x3F80, 0x3F80, 0x3F80};
    int aoff[2];
#pragma unroll
    for (int ks = 0; ks < 2; ++ks) aoff[ks] = l15 * 128 + (((4 * ks + g4) ^ (l15 & 7)) << 4);
    const int fl = ((lane >> 3) & 1) | (((lane >> 4) & 1) << 1);
    const int boff = K1_AST + (8 * g4 + (l15 >> 2)) * 128 + 8 * (lane & 3) + ((hw ^ fl) << 5);
    f32x4 sm = {0.f, 0.f, 0.f, 0.f}, sg = sm, sm4 = sm, sg4 = sm;
    bf16x8 af[2][5], bf[2][3], sf[2];
    auto rdfr = [&](int ks, int buf) {
      const char* sb = smem + buf * K1_ST;
#pragma unroll
      for (int pn = 0; pn < 3; ++pn) bf[ks][pn] = lds_tr2(sb + boff + pn * 8192 + ks * 4096);
#pragma unroll
      for (int mt = 0; mt < 5; ++mt) af[ks][mt] = lds_b128(sb + mt * 2048 + aoff[ks]);
      sf[ks] = lds_b128(sb + hw * 2048 + aoff[ks]);
    };
    auto mm = [&](int ks) {
#pragma unroll
      for (int pn = 0; pn < 3; ++pn) cs[pn] = mfma(bf[ks][pn], ones, cs[pn]);
#pragma unroll
      for (int mt = 0; mt < 5; ++mt) {
        if (mt > 0) acc[mt - 1] = mfma(bf[ks][0], af[ks][mt], acc[mt - 1]);
        acc[4 + mt] = mfma(bf[ks][1], af[ks][mt], acc[4 + mt]);
        acc[9 + mt] = mfma(bf[ks][2], af[ks][mt], acc[9 + mt]);
      }
      sm = mfma(ones, sf[ks], sm); sg = mfma(sf[ks], sf[ks], sg);
      sm4 = mfma(ones, af[ks][4], sm4); sg4 = mfma(af[ks][4], af[ks][4], sg4);
    };
    STAMP(1);
    ring_consumer<8, K1_NST, 12, 21>(rdfr, mm);
    STAMP(3);
    {
      const f32x2 st = stat_fin(sm, sg, lane), st4 = stat_fin(sm4, sg4, lane);
      if (lane < 16) { sst[hw * 16 + lane] = st; if (hw == 0) sst[64 + lane] = st4; }
    }
    bar();
    STAMP(4);
    char* sw = smem + hw * HSZ;
    char* sq = sw; char* sk = sw + 2 * QH; char* sv = sk + 2 * KH;
    const int half = g4 >> 1, o8 = (g4 & 1) * 8;
#pragma unroll
    for (int mt = 0; mt < 5; ++mt) {
      const f32x2 ms = sst[mt * 16 + l15];
      auto put = [&](char* base, int hs, int row, const f32x4& a, const f32x4& c) {
        u32x2 pq = {pack2((a[0] - ms[0] * c[0]) * ms[1], (a[1] - ms[0] * c[1]) * ms[1]),
                    pack2((a[2] - ms[0] * c[2]) * ms[1], (a[3] - ms[0] * c[3]) * ms[1])};
        *(u32x2*)(base + half * hs + row * 16 + o8) = pq;
      };
      if (mt > 0) put(sq, QH, (mt - 1) * 16 + l15, acc[mt - 1], cs[0]);
      put(sk, KH, mt * 16 + l15, acc[4 + mt], cs[1]);
      put(sv, KH, mt * 16 + l15, acc[9 + mt], cs[2]);
    }
  }
  bar();
  {
    const int hf = lane & 1, tl = mh * 32 + (lane >> 1);
    const int t = r0 + tl;
    const char* sq = smem + hw * HSZ + hf * QH;
    const char* sk = smem + hw * HSZ + 2 * QH + hf * KH;
    const char* sv = sk + 2 * KH;
    u32x4 q0 = *(const u32x4*)(sq + tl * 16);
    float lg[8];
    float mx = -3.0e38f;
#pragma unroll
    for (int j = 0; j < 8; ++j) {
      u32x4 k0 = *(const u32x4*)(sk + (tl + 16 - 2 * j) * 16);
      float d = 0.f;
#pragma unroll
      for (int e = 0; e < 4; ++e) d = dot2(q0[e], k0[e], d);
      d += dpp_f<0xB1>(d);
      d = rbf(d) * 0.25f;
      d = (t - 2 * j >= 0) ? d : -1.0e9f;
      lg[j] = d;
      mx = fmaxf(mx, d);
    }
    float sum = 0.f;
#pragma unroll
    for (int j = 0; j < 8; ++j) { lg[j] = __expf(lg[j] - mx); sum += lg[j]; }
    const float rs = 1.0f / sum;
    float ov[8];
#pragma unroll
    for (int e = 0; e < 8; ++e) ov[e] = 0.f;
#pragma unroll
    for (int jp = 0; jp < 4; ++jp) {
      const u32 pp = pack2(lg[2 * jp] * rs, lg[2 * jp + 1] * rs);
      int ea = tl + 16 - 4 * jp;
      u32x4 va = *(const u32x4*)(sv + ea * 16), vb = *(const u32x4*)(sv + (ea - 2) * 16);
#pragma unroll
      for (int e = 0; e < 4; ++e) {
        u32 lo = __builtin_amdgcn_perm(vb[e], va[e], 0x05040100u);
        u32 hi = __builtin_amdgcn_perm(vb[e], va[e], 0x07060302u);
        ov[2 * e] = dot2(lo, pp, ov[2 * e]);
        ov[2 * e + 1] = dot2(hi, pp, ov[2 * e + 1]);
      }
    }
    u32x4 w0;
#pragma unroll
    for (int e = 0; e < 4; ++e) w0[e] = pack2(ov[2 * e], ov[2 * e + 1]);
    *(u32x4*)(o + (size_t)t * D_ + hg * 64 + hw * 16 + hf * 8) = w0;
  }
  STAMP(5);
  STAMP_OUT(0, 6);
}

template <int LDA, int NT, int KW, int NST>
__global__ __launch_bounds__(256, 1) void k_proj_res(const u16* __restrict__ A, const u16* __restrict__ W,
                                                     const u16* __restrict__ res, u16* __restrict__ out) {
  constexpr int ST = 16384;
  __shared__ __attribute__((aligned(16))) char smem[NST * ST];
  const int tid = threadIdx.x, lane = tid & 63;
  const int wid = __builtin_amdgcn_readfirstlane(tid >> 6);
  const int bid = blockIdx.x, xcd = bid & 7, idx = bid >> 3;
  const int mt0 = (xcd >> 1) * 8 + (idx & 7), nt0 = (xcd & 1) * 4 + (idx >> 3);
  const int r0 = mt0 * 64, c0 = nt0 * 64;
  const int l15 = lane & 15, g4 = lane >> 4;
  const int wm = wid >> 1, wn = wid & 1;
  const u32 lb = lds_addr(smem);
  STAMP_DECL STAMP(0);

  u32x2 rv[2][2];
#pragma unroll
  for (int i = 0; i < 2; ++i)
#pragma unroll
    for (int j = 0; j < 2; ++j)
      rv[i][j] = *(const u32x2*)(res + (size_t)(r0 + wm * 32 + i * 16 + l15) * D_ + c0 + wn * 32 + j * 16 + 4 * g4);

  auto load = [&](int kt, int buf) {
#pragma unroll
    for (int i = 0; i < 2; ++i) {
      int p = wid * 2 + i;
      int sc = (lane & 7) ^ (lane >> 3);
      glds16(A + (size_t)(r0 + 8 * p) * LDA + kt * 64, (u32)((lane >> 3) * (LDA * 2) + sc * 16), lb + buf * ST + p * 1024);
    }
#pragma unroll
    for (int i = 0; i < 2; ++i) {
      int p = wid * 2 + i;
      int f = ((lane >> 4) & 1) | ((p & 1) << 1);
      int sc = ((((lane & 7) >> 1) ^ f) << 1) | (lane & 1);
      int krow = kt * 64 + 8 * p + (lane >> 3);
      krow = krow < KW ? krow : KW - 1;
      glds16(W + c0, (u32)(krow * (D_ * 2) + sc * 16), lb + buf * ST + 8192 + p * 1024);
    }
  };
  sfor<NST - 1>([&](auto i) { load(decltype(i)::v, decltype(i)::v); });
  STAMP(1);

  f32x4 acc[2][2];
#pragma unroll
  for (int i = 0; i < 2; ++i)
#pragma unroll
    for (int j = 0; j < 2; ++j) acc[i][j] = (f32x4){0.f, 0.f, 0.f, 0.f};
  int aoff[2], boff[2];
  const int fl = ((lane >> 3) & 1) | (((lane >> 4) & 1) << 1);
#pragma unroll
  for (int ks = 0; ks < 2; ++ks) aoff[ks] = (wm * 32 + l15) * 128 + (((4 * ks + g4) ^ (l15 & 7)) << 4);
#pragma unroll
  for (int j = 0; j < 2; ++j) boff[j] = 8192 + (8 * g4 + (l15 >> 2)) * 128 + 8 * (lane & 3) + (((wn * 2 + j) ^ fl) << 5);

  sfor<NT>([&](auto ktc) {
    constexpr int kt = decltype(ktc)::v;
    constexpr int rem = NT - 1 - kt;
    wait_vm<4 * (rem < NST - 2 ? rem : NST - 2)>();
    bar();
    STAMP(2 + kt);
    if constexpr (kt + NST - 1 < NT) load(kt + NST - 1, (kt + NST - 1) % NST);
    const char* sb = smem + (kt % NST) * ST;
#pragma unroll
    for (int ks = 0; ks < 2; ++ks) {
      bf16x8 af[2], bf[2];
#pragma unroll
      for (int i = 0; i < 2; ++i) af[i] = lds_b128(sb + i * 2048 + aoff[ks]);
#pragma unroll
      for (int j = 0; j < 2; ++j) bf[j] = lds_tr2(sb + boff[j] + ks * 4096);
#pragma unroll
      for (int i = 0; i < 2; ++i)
#pragma unroll
        for (int j = 0; j < 2; ++j) acc[i][j] = mfma(bf[j], af[i], acc[i][j]);
    }
  });
#pragma unroll
  for (int i = 0; i < 2; ++i)
#pragma unroll
    for (int j = 0; j < 2; ++j) {
      u32x2 r = rv[i][j], ov;
      ov[0] = pack2(bflo(r[0]) + rbf(acc[i][j][0]), bfhi(r[0]) + rbf(acc[i][j][1]));
      ov[1] = pack2(bflo(r[1]) + rbf(acc[i][j][2]), bfhi(r[1]) + rbf(acc[i][j][3]));
      *(u32x2*)(out + (size_t)(r0 + wm * 32 + i * 16 + l15) * D_ + c0 + wn * 32 + j * 16 + 4 * g4) = ov;
    }
  STAMP(2 + NT);
  STAMP_OUT((NT == 8 ? 1 : 4), 3 + NT);
}

constexpr int K3_AST = 64 * 128;
constexpr int K3_ST = K3_AST + 64 * 192 * 2;
constexpr int K3_NST = 4;

constexpr int K3E_ACC = 0;
constexpr int K3E_ST = 36864;
constexpr int K3E_FIN = 38912;
constexpr int K3E_OUT = 40960;

template <bool Z2>
DEVI void k3_epi(char* smem, f32x4 (&acc)[2][3], const f32x4 (&cs)[3], u16* __restrict__ z1, u16* __restrict__ z2t,
                 u64* __restrict__ gran, int rt, int ct, int lane, int wn, int mt0, int tid) {
  const int r0 = rt * 64;
  const int l15 = lane & 15, g4 = lane >> 4;
  const f32x2* sst = (const f32x2*)(smem + K3_NST * K3_ST);
  const int u0 = ct * 12 + wn * 3;
  int nv = 43 - u0; nv = nv < 0 ? 0 : (nv > 3 ? 3 : nv);
  const int ncols = (ct < 3 ? 12 : 7) * 16;
  u32 zp[2][3][2];
#pragma unroll
  for (int i = 0; i < 2; ++i) {
    f32x2 mn[2], rs[2];
    if constexpr (Z2) {
      const f32x4 a = *(const f32x4*)(sst + (mt0 + i) * 16 + 4 * g4), b = *(const f32x4*)(sst + (mt0 + i) * 16 + 4 * g4 + 2);
      mn[0] = (f32x2){a[0], a[2]}; rs[0] = (f32x2){a[1], a[3]};
      mn[1] = (f32x2){b[0], b[2]}; rs[1] = (f32x2){b[1], b[3]};
    } else {
      const f32x2 ms = sst[(mt0 + i) * 16 + l15];
      mn[0] = mn[1] = (f32x2){ms[0], ms[0]}; rs[0] = rs[1] = (f32x2){ms[1], ms[1]};
    }
#pragma unroll
    for (int j = 0; j < 3; ++j)
#pragma unroll
      for (int hh = 0; hh < 2; ++hh) {
        f32x2 a = {acc[i][j][2 * hh], acc[i][j][2 * hh + 1]}, c = {cs[j][2 * hh], cs[j][2 * hh + 1]};
        f32x2 y = (a - mn[hh] * c) * rs[hh];
        f32x2 g = gelu2(unpk(pack2(y[0], y[1])));
        zp[i][j][hh] = pack2(g[0], g[1]);
      }
  }
  if constexpr (!Z2) {
    char* img = smem + K3E_OUT;
#pragma unroll
    for (int i = 0; i < 2; ++i)
#pragma unroll
      for (int j = 0; j < 3; ++j)
        *(u32x2*)(img + ((mt0 + i) * 16 + l15) * 400 + (wn * 48 + j * 16 + 4 * g4) * 2) = (u32x2){zp[i][j][0], zp[i][j][1]};
    bar();
#pragma unroll
    for (int it = 0; it < 3; ++it) {
      int c = tid + it * 512, row = c / 24, ch = c - row * 24;
      if (ch * 8 < ncols)
        *(u32x4*)(z1 + (size_t)(r0 + row) * FH + ct * 192 + ch * 8) = *(const u32x4*)(img + row * 400 + ch * 16);
    }
  } else {
    f32x2* st = (f32x2*)(smem + K3E_ST);
    f32x2* fin = (f32x2*)(smem + K3E_FIN);
    float sv[16];
#pragma unroll
    for (int i = 0; i < 2; ++i)
#pragma unroll
      for (int hh = 0; hh < 2; ++hh) {
        f32x2 s = {0.f, 0.f}, q = {0.f, 0.f};
#pragma unroll
        for (int j = 0; j < 3; ++j) {
          const f32x2 z = unpk(j < nv ? zp[i][j][hh] : 0u);
          s += z; q += z * z;
        }
        sv[(i * 2 + hh) * 4 + 0] = s[0]; sv[(i * 2 + hh) * 4 + 1] = q[0];
        sv[(i * 2 + hh) * 4 + 2] = s[1]; sv[(i * 2 + hh) * 4 + 3] = q[1];
      }
#pragma unroll
    for (int k = 0; k < 16; ++k) sv[k] += dpp_f<0xB1>(sv[k]);
#pragma unroll
    for (int k = 0; k < 16; ++k) sv[k] += dpp_f<0x4E>(sv[k]);
#pragma unroll
    for (int k = 0; k < 16; ++k) sv[k] += dpp_f<0x141>(sv[k]);
#pragma unroll
    for (int k = 0; k < 16; ++k) sv[k] += dpp_f<0x140>(sv[k]);
    if (l15 == 0) {
#pragma unroll
      for (int i = 0; i < 2; ++i)
#pragma unroll
        for (int hh = 0; hh < 2; ++hh)
          *(f32x4*)(st + wn * 64 + (mt0 + i) * 16 + 4 * g4 + 2 * hh) =
              (f32x4){sv[(i * 2 + hh) * 4], sv[(i * 2 + hh) * 4 + 1], sv[(i * 2 + hh) * 4 + 2], sv[(i * 2 + hh) * 4 + 3]};
    }
    bar();
    GSTAMP(2, 18);
    if (tid < 64) {
      const f32x2 p = st[tid] + st[64 + tid] + st[128 + tid] + st[192 + tid];
      u64* gbase = gran + (size_t)(rt * 4) * 128;
      __hip_atomic_store(gbase + ct * 128 + tid * 2, (1ull << 32) | (u64)__float_as_uint(p[0]), __ATOMIC_RELAXED, __HIP_MEMORY_SCOPE_AGENT);
      __hip_atomic_store(gbase + ct * 128 + tid * 2 + 1, (1ull << 32) | (u64)__float_as_uint(p[1]), __ATOMIC_RELAXED, __HIP_MEMORY_SCOPE_AGENT);
      float ts, tq;
      int spins = 0;
      bool ok;
      do {
        ok = true; ts = 0.f; tq = 0.f;
#pragma unroll
        for (int c = 0; c < 4; ++c) {
          u64 a = __hip_atomic_load(gbase + c * 128 + tid * 2, __ATOMIC_RELAXED, __HIP_MEMORY_SCOPE_AGENT);
          u64 b = __hip_atomic_load(gbase + c * 128 + tid * 2 + 1, __ATOMIC_RELAXED, __HIP_MEMORY_SCOPE_AGENT);
          ok = ok && (a >> 32) == 1ull && (b >> 32) == 1ull;
          ts += __uint_as_float((u32)a); tq += __uint_as_float((u32)b);
        }
      } while (!ok && ++spins < (1 << 20));
      const float tmean = ts * (1.0f / 688.0f);
      const float vb = rbf(fmaxf(tq * (1.0f / 688.0f) - tmean * tmean, 0.f));
      const float sd = rbf(__builtin_sqrtf(rbf(vb + 1e-5f)));
      fin[tid] = (f32x2){rbf(tmean), 1.0f / sd};
    }
    GSTAMP(2, 19);
    bar();
    GSTAMP(2, 20);
    char* img = smem + K3E_OUT;
#pragma unroll
    for (int i = 0; i < 2; ++i) {
      const int row = (mt0 + i) * 16 + 4 * g4;
      const f32x4 a = *(const f32x4*)(fin + row), b = *(const f32x4*)(fin + row + 2);
      const f32x2 mn0 = {a[0], a[2]}, rs0 = {a[1], a[3]}, mn1 = {b[0], b[2]}, rs1 = {b[1], b[3]};
#pragma unroll
      for (int j = 0; j < 3; ++j) {
        f32x2 d0 = unpk(zp[i][j][0]) - mn0, d1 = unpk(zp[i][j][1]) - mn1;
        d0 = unpk(pack2(d0[0], d0[1])) * rs0; d1 = unpk(pack2(d1[0], d1[1])) * rs1;
        *(u32x2*)(img + (wn * 48 + j * 16 + l15) * 144 + row * 2) = (u32x2){pack2(d0[0], d0[1]), pack2(d1[0], d1[1])};
      }
    }
    bar();
    GSTAMP(2, 21);
#pragma unroll
    for (int it = 0; it < 3; ++it) {
      int c = tid + it * 512, f = c >> 3, ch = c & 7;
      if (f < ncols)
        *(u32x4*)(z2t + (size_t)(ct * 192 + f) * S_ + r0 + ch * 8) = *(const u32x4*)(img + f * 144 + ch * 16);
    }
  }
}

template <bool Z2>
DEVI void k3_compute(char* smem, u16* __restrict__ z1, u16* __restrict__ z2t,
                     u64* __restrict__ gran, int rt, int ct, int lane, int wn, int tid) {
  STAMP_DECL STAMP(0);
  f32x2* sst = (f32x2*)(smem + K3_NST * K3_ST);
  f32x4 acc[2][3], acc2[2][3], cs[3];
#pragma unroll
  for (int i = 0; i < 2; ++i)
#pragma unroll
    for (int j = 0; j < 3; ++j) { acc[i][j] = (f32x4){0.f, 0.f, 0.f, 0.f}; acc2[i][j] = acc[i][j]; }
#pragma unroll
  for (int j = 0; j < 3; ++j) cs[j] = (f32x4){0.f, 0.f, 0.f, 0.f};
  const bf16x8 ones = {0x3F80, 0x3F80, 0x3F80, 0x3F80, 0x3F80, 0x3F80, 0x3F80, 0x3F80};
  const int l15 = lane & 15, g4 = lane >> 4;
  int aoff[2], boff[3];
#pragma unroll
  for (int ks = 0; ks < 2; ++ks) aoff[ks] = l15 * 128 + (((4 * ks + g4) ^ (l15 & 7)) << 4);
  const int fl = ((lane >> 3) & 1) | (((lane >> 4) & 1) << 1);
#pragma unroll
  for (int j = 0; j < 3; ++j) {
    int nt = wn * 3 + j;
    boff[j] = K3_AST + (nt >> 2) * 8192 + (8 * g4 + (l15 >> 2)) * 128 + 8 * (lane & 3) + (((nt & 3) ^ fl) << 5);
  }
  f32x4 sm = {0.f, 0.f, 0.f, 0.f}, sg = sm;
  bf16x8 af[2][4], bf[2][3], sf[2];
  auto rdfr = [&](int ks, int buf) {
    const char* sb = smem + buf * K3_ST;
#pragma unroll
    for (int j = 0; j < 3; ++j) bf[ks][j] = lds_tr2(sb + boff[j] + ks * 4096);
#pragma unroll
    for (int mt = 0; mt < 4; ++mt) af[ks][mt] = lds_b128(sb + mt * 2048 + aoff[ks]);
    sf[ks] = lds_b128(sb + wn * 2048 + aoff[ks]);
  };
  auto mm = [&](int ks) {
#pragma unroll
    for (int j = 0; j < 3; ++j) {
      if constexpr (Z2) cs[j] = mfma(ones, bf[ks][j], cs[j]);
      else cs[j] = mfma(bf[ks][j], ones, cs[j]);
    }
#pragma unroll
    for (int mt = 0; mt < 2; ++mt)
#pragma unroll
      for (int j = 0; j < 3; ++j) {
        if constexpr (Z2) { acc[mt][j] = mfma(af[ks][mt], bf[ks][j], acc[mt][j]); acc2[mt][j] = mfma(af[ks][2 + mt], bf[ks][j], acc2[mt][j]); }
        else { acc[mt][j] = mfma(bf[ks][j], af[ks][mt], acc[mt][j]); acc2[mt][j] = mfma(bf[ks][j], af[ks][2 + mt], acc2[mt][j]); }
      }
    sm = mfma(ones, sf[ks], sm); sg = mfma(sf[ks], sf[ks], sg);
  };
  STAMP(1);
  ring_consumer<8, K3_NST, 11, 17>(rdfr, mm, 2);
  STAMP(3);
  bar();
  {
    const f32x2 st = stat_fin(sm, sg, lane);
    if (lane < 16) sst[wn * 16 + lane] = st;
    f32x4* ho = (f32x4*)(smem + K3E_ACC) + wn * 9 * 64 + lane;
#pragma unroll
    for (int i = 0; i < 2; ++i)
#pragma unroll
      for (int j = 0; j < 3; ++j) ho[(i * 3 + j) * 64] = acc2[i][j];
#pragma unroll
    for (int j = 0; j < 3; ++j) ho[(6 + j) * 64] = cs[j];
  }
  bar();
  STAMP(4);
  k3_epi<Z2>(smem, acc, cs, z1, z2t, gran, rt, ct, lane, wn, 0, tid);
  STAMP(5);
  STAMP_OUT(2, 6);
}

template <bool Z2>
DEVI void k3_helper(char* smem, u16* __restrict__ z1, u16* __restrict__ z2t,
                    u64* __restrict__ gran, int rt, int ct, int lane, int wn, int tid) {
  bar();
  bar();
  f32x4 acc[2][3], cs[3];
  const f32x4* hi = (const f32x4*)(smem + K3E_ACC) + wn * 9 * 64 + lane;
#pragma unroll
  for (int i = 0; i < 2; ++i)
#pragma unroll
    for (int j = 0; j < 3; ++j) acc[i][j] = hi[(i * 3 + j) * 64];
#pragma unroll
  for (int j = 0; j < 3; ++j) cs[j] = hi[(6 + j) * 64];
  k3_epi<Z2>(smem, acc, cs, z1, z2t, gran, rt, ct, lane, wn, 2, tid);
}

__global__ __launch_bounds__(512, 2) void k3_in_gelu(const u16* __restrict__ x, const u16* __restrict__ w,
                                                     u16* __restrict__ z1, u16* __restrict__ z2t,
                                                     u64* __restrict__ gran) {
  __shared__ __attribute__((aligned(16))) char smem[K3_NST * K3_ST + 1024];
  const int tid = threadIdx.x, lane = tid & 63;
  const int wid = __builtin_amdgcn_readfirstlane(tid >> 6);
  const int bid = blockIdx.x, xcd = bid & 7, idx = bid >> 3;
  const int rt = (xcd >> 1) * 8 + (idx & 7), c8 = (xcd & 1) * 4 + (idx >> 3);
  const int r0 = rt * 64;
  const u32 lb = lds_addr(smem);
  if (wid >= 4) {
    const int lw = wid - 4;
    const int cbase = (c8 >> 2) * FH + (c8 & 3) * 192;
    u32 bvoff[6];
#pragma unroll
    for (int i = 0; i < 6; ++i) {
      int pi = lw * 6 + i, panel = pi >> 3, p = pi & 7;
      int f = ((lane >> 4) & 1) | ((p & 1) << 1);
      int sc = ((((lane & 7) >> 1) ^ f) << 1) | (lane & 1);
      int col = cbase + panel * 64 + sc * 8;
      col = col <= F2 - 8 ? col : F2 - 8;
      bvoff[i] = (u32)((lane >> 3) * (F2 * 2) + col * 2);
    }
    const u32 avoff = (u32)((lane >> 3) * (D_ * 2) + (((lane & 7) ^ (lane >> 3)) << 4));
    ring_loader<8, K3_NST, 8, 2>([&](int kt, int buf) {
#pragma unroll
      for (int i = 0; i < 2; ++i)
        glds16(x + (size_t)(r0 + 8 * (lw * 2 + i)) * D_ + kt * 64, avoff, lb + buf * K3_ST + (lw * 2 + i) * 1024);
#pragma unroll
      for (int i = 0; i < 6; ++i) {
        int pi = lw * 6 + i, panel = pi >> 3, p = pi & 7;
        glds16(w + (size_t)(kt * 64 + 8 * p) * F2, bvoff[i], lb + buf * K3_ST + K3_AST + panel * 8192 + p * 1024);
      }
    });
    if (c8 < 4) k3_helper<false>(smem, z1, z2t, gran, rt, c8, lane, lw, tid);
    else k3_helper<true>(smem, z1, z2t, gran, rt, c8 - 4, lane, lw, tid);
    return;
  }
  if (c8 < 4) k3_compute<false>(smem, z1, z2t, gran, rt, c8, lane, wid, tid);
  else k3_compute<true>(smem, z1, z2t, gran, rt, c8 - 4, lane, wid, tid);
}

constexpr int K4_ST = (64 + 96) * 128;
constexpr int K4_NST = 8;
constexpr int K4_NT = 32;

__global__ __launch_bounds__(512, 2) void k4_spatial(const u16* __restrict__ wsp, const u16* __restrict__ z2t,
                                                     const u16* __restrict__ z1, const u16* __restrict__ bsp,
                                                     u16* __restrict__ h) {
  __shared__ __attribute__((aligned(16))) char smem[K4_NST * K4_ST];
  const int tid = threadIdx.x, lane = tid & 63;
  const int wid = __builtin_amdgcn_readfirstlane(tid >> 6);
  const int bid = blockIdx.x, xcd = bid & 7, idx = bid >> 3;
  const int mt0 = (xcd >> 1) * 8 + (idx & 7), nt0 = (xcd & 1) * 4 + (idx >> 3);
  const int r0 = mt0 * 64;
  const int u0 = nt0 * 6 < 37 ? nt0 * 6 : 37;
  const int f0 = u0 * 16;
  STAMP_DECL STAMP(0);
  if (wid >= 4) {
    const int lw = wid - 4;
    const u16* src[5];
#pragma unroll
    for (int i = 0; i < 5; ++i) {
      int pi = lw * 5 + i;
      src[i] = (pi < 8 ? wsp + (size_t)(r0 + 8 * pi) * S_ : z2t + (size_t)(f0 + 8 * (pi - 8)) * S_);
    }
    const u32 lb = lds_addr(smem) + lw * 5 * 1024;
    const u32 voff = (u32)((lane >> 3) * (S_ * 2) + (((lane & 7) ^ (lane >> 3)) << 4));
    auto load = [&](int kt, int buf) {
#pragma unroll
      for (int i = 0; i < 5; ++i) glds16(src[i] + kt * 64, voff, lb + buf * K4_ST + i * 1024);
    };
    ring_loader<K4_NT, K4_NST, 5, K4_NST - 1>(load);
    return;
  }
  const int l15 = lane & 15, g4 = lane >> 4;
  const int wm = wid >> 1, wn = wid & 1;
  u32x2 zv[2][3];
  float bs[2];
#pragma unroll
  for (int i = 0; i < 2; ++i) {
    bs[i] = bf2f(bsp[r0 + wm * 32 + i * 16 + l15]);
#pragma unroll
    for (int j = 0; j < 3; ++j)
      zv[i][j] = *(const u32x2*)(z1 + (size_t)(r0 + wm * 32 + i * 16 + l15) * FH + f0 + wn * 48 + j * 16 + 4 * g4);
  }
  f32x4 acc[2][3];
#pragma unroll
  for (int i = 0; i < 2; ++i)
#pragma unroll
    for (int j = 0; j < 3; ++j) acc[i][j] = (f32x4){0.f, 0.f, 0.f, 0.f};
  int aoff[2], boff[2];
#pragma unroll
  for (int ks = 0; ks < 2; ++ks) {
    aoff[ks] = (wm * 32 + l15) * 128 + (((4 * ks + g4) ^ (l15 & 7)) << 4);
    boff[ks] = 8192 + (wn * 48 + l15) * 128 + (((4 * ks + g4) ^ (l15 & 7)) << 4);
  }
  bf16x8 af[2][2], bf[2][3];
  auto rdfr = [&](int ks, int buf) {
    const char* sb = smem + buf * K4_ST;
#pragma unroll
    for (int i = 0; i < 2; ++i) af[ks][i] = lds_b128(sb + i * 2048 + aoff[ks]);
#pragma unroll
    for (int j = 0; j < 3; ++j) bf[ks][j] = lds_b128(sb + j * 2048 + boff[ks]);
  };
  auto mm = [&](int ks) {
#pragma unroll
    for (int i = 0; i < 2; ++i)
#pragma unroll
      for (int j = 0; j < 3; ++j) acc[i][j] = mfma(bf[ks][j], af[ks][i], acc[i][j]);
  };
  STAMP(1);
  ring_consumer<K4_NT, K4_NST, 5, 6>(rdfr, mm, 3);
  STAMP(6);
  const int umin = nt0 * 6;
#pragma unroll
  for (int i = 0; i < 2; ++i) {
    u16* hp = h + (size_t)(r0 + wm * 32 + i * 16 + l15) * FHP;
#pragma unroll
    for (int j = 0; j < 3; ++j) {
      int u = u0 + wn * 3 + j;
      if (u >= umin) {
        u32x2 z = zv[i][j], ov;
        float g0 = rbf(rbf(acc[i][j][0]) + bs[i]), g1 = rbf(rbf(acc[i][j][1]) + bs[i]);
        float g2 = rbf(rbf(acc[i][j][2]) + bs[i]), g3 = rbf(rbf(acc[i][j][3]) + bs[i]);
        ov[0] = pack2(bflo(z[0]) * g0, bfhi(z[0]) * g1);
        ov[1] = pack2(bflo(z[1]) * g2, bfhi(z[1]) * g3);
        *(u32x2*)(hp + u * 16 + 4 * g4) = ov;
      }
    }
    if (nt0 == 7 && wn == 1) *(u32x2*)(hp + FH + 4 * g4) = (u32x2){0u, 0u};
  }
  STAMP(7);
  STAMP_OUT(3, 8);
}

extern "C" void kernel_launch(void* const* d_in, const int* in_sizes, int n_in, void* d_out, int out_size,
                              void* d_ws, size_t ws_size, hipStream_t stream) {
  const u16* x = (const u16*)d_in[0];
  const u16* wqkv = (const u16*)d_in[1];
  const u16* wo = (const u16*)d_in[2];
  const u16* w_in = (const u16*)d_in[4];
  const u16* w_sp = (const u16*)d_in[5];
  const u16* b_sp = (const u16*)d_in[6];
  const u16* w_out = (const u16*)d_in[7];
  u16* out = (u16*)d_out;
  char* ws = (char*)d_ws;
  constexpr size_t MB = 1 << 20;
  u64* gran = (u64*)ws;
  u16* o = (u16*)(ws + 1 * MB);
  u16* x1 = (u16*)(ws + 4 * MB);
  u16* x2 = (u16*)(ws + 7 * MB);
  u16* z1 = (u16*)(ws + 10 * MB);
  u16* z2t = (u16*)(ws + 14 * MB);
  u16* h = (u16*)(ws + 18 * MB);
  const int gran_words = 2 * 32 * 4 * 128 * 2;

  for (int L = 0; L < 2; ++L) {
    const u16* xin = L == 0 ? x : x2;
    u16* xout = L == 0 ? x2 : out;
    k1_qkv_attn<<<256, 512, 0, stream>>>(xin, wqkv, o, (u32*)gran, L == 0 ? gran_words : 0);
    k_proj_res<512, 8, 512, 8><<<256, 256, 0, stream>>>(o, wo, xin, x1);
    k3_in_gelu<<<256, 512, 0, stream>>>(x1, w_in, z1, z2t, gran + (size_t)L * 32 * 4 * 128);
    k4_spatial<<<256, 512, 0, stream>>>(w_sp, z2t, z1, b_sp, h);
    k_proj_res<FHP, 11, FH, 8><<<256, 256, 0, stream>>>(h, w_out, x1, xout);
  }
}
```

```cpp
#include <hip/hip_runtime.h>
#include <hip/hip_bf16.h>
#include <stdint.h>

typedef __attribute__((ext_vector_type(8))) short bf16x8;
typedef __attribute__((ext_vector_type(4))) short s16x4;
typedef __attribute__((ext_vector_type(4))) float f32x4;
typedef __attribute__((ext_vector_type(2))) float f32x2;
typedef __attribute__((ext_vector_type(2))) __bf16 bf16v2;
typedef __attribute__((ext_vector_type(4))) unsigned u32x4;
typedef __attribute__((ext_vector_type(2))) unsigned u32x2;
typedef unsigned short u16;
typedef unsigned int u32;
typedef unsigned long long u64;

#define LDS_AS __attribute__((address_space(3)))
#define GLB_AS __attribute__((address_space(1)))
#define DEVI __device__ __forceinline__

constexpr int S_ = 2048, D_ = 512, FH = 688, FHP = 704, F2 = 1376;

DEVI float bf2f(u32 v) { return __uint_as_float(v << 16); }
DEVI float bflo(u32 w) { return __uint_as_float(w << 16); }
DEVI float bfhi(u32 w) { return __uint_as_float(w & 0xffff0000u); }
DEVI u32 pack2(float a, float b) {
  bf16v2 v; v[0] = (__bf16)a; v[1] = (__bf16)b;
  return __builtin_bit_cast(u32, v);
}
DEVI float dot2(u32 a, u32 b, float c) {
  return __builtin_amdgcn_fdot2_f32_bf16(__builtin_bit_cast(bf16v2, a), __builtin_bit_cast(bf16v2, b), c, false);
}
DEVI float rbf(float f) { return bf2f((u32)__builtin_bit_cast(u16, (__bf16)f)); }

DEVI void glds16(const void* sbase, u32 voff, u32 lds) {
  u32 keep;
  asm volatile("s_mov_b32 %0, m0\n\ts_mov_b32 m0, %3\n\ts_nop 0\n\tglobal_load_lds_dwordx4 %1, %2\n\ts_mov_b32 m0, %0"
               : "=&s"(keep) : "v"(voff), "s"(sbase), "s"(lds) : "memory");
}
DEVI u32 lds_addr(const void* p) { return (u32)(size_t)(LDS_AS const char*)p; }
template <int N> DEVI void wait_vm() { asm volatile("s_waitcnt vmcnt(%0)" ::"n"(N) : "memory"); }
#define GSTAMP(k, i)
#define GS2(i)
DEVI void wait_lgkm0() {
  __builtin_amdgcn_sched_barrier(0);
  __builtin_amdgcn_s_waitcnt(0xC07F);
  asm volatile("" ::: "memory");
  __builtin_amdgcn_sched_barrier(0);
}
DEVI void bar() { wait_lgkm0(); __builtin_amdgcn_s_barrier(); __builtin_amdgcn_sched_barrier(0); }

DEVI void st8(void* p, u32x2 v) {
  __hip_atomic_store((u64*)p, ((u64)v[1] << 32) | (u64)v[0], __ATOMIC_RELAXED, __HIP_MEMORY_SCOPE_AGENT);
}
DEVI void st16(void* p, u32x4 v) {
  asm volatile("global_store_dwordx4 %0, %1, off sc1\n\ts_nop 1" ::"v"(p), "v"(v) : "memory");
}
DEVI void publish(u32* cnt) {
  wait_vm<0>();
  bar();
  if (cnt != nullptr && threadIdx.x == 0) __hip_atomic_fetch_add(cnt, 1u, __ATOMIC_RELAXED, __HIP_MEMORY_SCOPE_AGENT);
}
DEVI void dep_wait(const u32* cnt) {
  int spins = 0;
  while (__hip_atomic_load(cnt, __ATOMIC_RELAXED, __HIP_MEMORY_SCOPE_AGENT) < 8u && ++spins < (1 << 21))
    __builtin_amdgcn_s_sleep(1);
}

template <int I> struct IC { static constexpr int v = I; };
template <int N, int I = 0, class F> DEVI void sfor(F&& f) {
  if constexpr (I < N) { f(IC<I>{}); sfor<N, I + 1>(f); }
}

template <int NRD, int NMM> DEVI void interleave() {
  constexpr int NP = NRD < NMM ? NRD : NMM;
#pragma unroll
  for (int i = 0; i < NP; ++i) {
    __builtin_amdgcn_sched_group_barrier(0x008, 1, 0);
    __builtin_amdgcn_sched_group_barrier(0x100, 1, 0);
  }
  if constexpr (NMM > NP) __builtin_amdgcn_sched_group_barrier(0x008, NMM - NP, 0);
  if constexpr (NRD > NP) __builtin_amdgcn_sched_group_barrier(0x100, NRD - NP, 0);
}
template <int NT, int NST, int NRD, int NMM, class Rd, class Mm> DEVI void ring_consumer(Rd&& rdfr, Mm&& mm) {
  bar();
  GS2(0);
  bar();
  GS2(1);
  rdfr(0, 0);
  int rd = 0;
#pragma unroll 1
  for (int kt = 0; kt < NT - 1; ++kt) {
    wait_lgkm0();
    rdfr(1, rd);
    mm(0);
    interleave<NRD, NMM>();
    rd = rd + 1 == NST ? 0 : rd + 1;
    bar();
    rdfr(0, rd);
    mm(1);
    interleave<NRD, NMM>();
  }
  wait_lgkm0();
  rdfr(1, rd);
  mm(0);
  interleave<NRD, NMM>();
  wait_lgkm0();
  mm(1);
  GS2(2);
}
template <int NT, int NST, int LA, int LB, int PRE, class LdA, class LdB> DEVI void ring_loader(LdA&& ldA, LdB&& ldB) {
  static_assert(PRE + 1 <= NST && PRE + 1 <= NT, "ring");
  constexpr int LPW = LA + LB;
#pragma unroll 1
  for (int i = 0; i < PRE; ++i) ldB(i, i);
  bar();
#pragma unroll 1
  for (int i = 0; i < PRE; ++i) ldA(i, i);
  wait_vm<LA * (PRE - 1)>();
  bar();
  ldA(PRE, PRE); ldB(PRE, PRE);
  int wr = PRE + 1 == NST ? 0 : PRE + 1;
  sfor<PRE - 1>([&](auto ic) {
    constexpr int kt = decltype(ic)::v;
    constexpr int nfull = (kt + 1 < NT - PRE ? kt + 1 : NT - PRE);
    wait_vm<LA * (PRE - 2 - kt) + LPW * nfull>();
    bar();
    if constexpr (kt + PRE + 1 < NT) {
      ldA(kt + PRE + 1, wr); ldB(kt + PRE + 1, wr);
      wr = wr + 1 == NST ? 0 : wr + 1;
    }
  });
#pragma unroll 1
  for (int kt = PRE - 1; kt < NT - 1 - PRE; ++kt) {
    wait_vm<LPW * (PRE - 1)>();
    bar();
    ldA(kt + PRE + 1, wr); ldB(kt + PRE + 1, wr);
    wr = wr + 1 == NST ? 0 : wr + 1;
  }
  constexpr int K0 = (PRE - 1 > NT - 1 - PRE) ? PRE - 1 : NT - 1 - PRE;
  sfor<NT - 1 - K0>([&](auto ic) {
    constexpr int kt = K0 + decltype(ic)::v;
    wait_vm<LPW * (NT - 2 - kt)>();
    bar();
  });
}

template <int CTRL> DEVI float dpp_f(float x) {
  return __builtin_bit_cast(float, __builtin_amdgcn_update_dpp(0, __builtin_bit_cast(int, x), CTRL, 0xF, 0xF, true));
}
DEVI float row16_sum(float x) {
  x += dpp_f<0xB1>(x);
  x += dpp_f<0x4E>(x);
  x += dpp_f<0x141>(x);
  x += dpp_f<0x140>(x);
  return x;
}

DEVI bf16x8 lds_b128(const char* p) { return *(const bf16x8*)p; }
DEVI bf16x8 lds_tr2(const char* p) {
  s16x4 a = __builtin_amdgcn_ds_read_tr16_b64_v4i16((LDS_AS s16x4*)p);
  s16x4 b = __builtin_amdgcn_ds_read_tr16_b64_v4i16((LDS_AS s16x4*)(p + 512));
  return __builtin_shufflevector(a, b, 0, 1, 2, 3, 4, 5, 6, 7);
}
DEVI f32x4 mfma(bf16x8 a, bf16x8 b, f32x4 c) { return __builtin_amdgcn_mfma_f32_16x16x32_bf16(a, b, c, 0, 0, 0); }

DEVI f32x2 gelu2(f32x2 x) {
  const f32x2 c1 = {-2.3022081985f, -2.3022081985f};
  const f32x2 c2 = {-0.10294324f, -0.10294324f};
  const f32x2 one = {1.0f, 1.0f};
  f32x2 a = x * (x * x * c2 + c1);
  f32x2 e = {__builtin_amdgcn_exp2f(a[0]), __builtin_amdgcn_exp2f(a[1])};
  f32x2 d = e + one;
  f32x2 r = {__builtin_amdgcn_rcpf(d[0]), __builtin_amdgcn_rcpf(d[1])};
  return x * r;
}
DEVI f32x2 unpk(u32 w) { return (f32x2){bflo(w), bfhi(w)}; }

DEVI f32x2 stat_fin(const f32x4& sm, const f32x4& sg, int lane) {
  const int j = lane & 3;
  float dg = j == 0 ? sg[0] : (j == 1 ? sg[1] : (j == 2 ? sg[2] : sg[3]));
  const float q = __shfl(dg, (lane & 15) + 16 * ((lane & 15) >> 2));
  const float mean = sm[0] * (1.0f / 512.0f);
  const float var = fmaxf(q * (1.0f / 512.0f) - mean * mean, 0.f);
  const float sd = rbf(__builtin_sqrtf(rbf(rbf(var) + 1e-5f)));
  return (f32x2){rbf(mean), 1.0f / sd};
}

struct Params {
  const u16 *wqkv, *wo, *w_in, *w_sp, *b_sp, *w_out;
  const u16 *z2t_in, *z1_in, *x1_in;
  u16 *h, *xout;
  const u16* xin;
  u16 *o, *x1, *z1, *z2t;
  u64* gran;
  u32* cnt;
  u32* zero_area;
  int zero_words, phases;
};
struct Ctx { int tid, lane, wid, rt, c8; u32 lb; };

constexpr int K1_AST = 80 * 128;
constexpr int K1_ST = K1_AST + 64 * 192 * 2;
constexpr int K1_NST = 4;

DEVI void phase1(char* smem, const Params& p, const Ctx& c, const u32* dep0, const u32* dep1, u32* sig) {
  f32x2* sst = (f32x2*)(smem + K1_NST * K1_ST);
  const u16* __restrict__ x = p.xin;
  const u16* __restrict__ w = p.wqkv;
  const int tid = c.tid, lane = c.lane, wid = c.wid, hg = c.c8;
  const int r0 = c.rt * 64;
  const u32 lb = c.lb;
  const int hw = wid & 3, mh = wid >> 2;
  const int l15 = lane & 15, g4 = lane >> 4;
  constexpr int QH = 1152, KH = 1408, HSZ = 2 * QH + 4 * KH;
  if (wid >= 4) {
    const int lw = wid - 4;
    u32 bvoff[2];
#pragma unroll
    for (int i = 0; i < 2; ++i) {
      int f = ((lane >> 4) & 1) | (i << 1);
      int sc = ((((lane & 7) >> 1) ^ f) << 1) | (lane & 1);
      bvoff[i] = (u32)((lane >> 3) * 3072 + sc * 16);
    }
    const int ap0 = lw < 2 ? lw * 3 : 6 + (lw - 2) * 2;
    u32 avoff[3];
#pragma unroll
    for (int i = 0; i < 3; ++i) {
      int gr = r0 - 16 + 8 * (ap0 + i) + (lane >> 3); gr = gr < 0 ? 0 : (gr > S_ - 1 ? S_ - 1 : gr);
      avoff[i] = (u32)(gr * (D_ * 2) + (((lane & 7) ^ (lane >> 3)) << 4));
    }
    auto ldB = [&](int kt, int buf) {
#pragma unroll
      for (int i = 0; i < 6; ++i) {
        int pi = lw * 6 + i, panel = pi >> 3, pp = pi & 7;
        glds16(w + (size_t)(kt * 64 + 8 * pp) * 1536 + panel * 512 + hg * 64, bvoff[i & 1],
               lb + buf * K1_ST + K1_AST + panel * 8192 + pp * 1024);
      }
    };
    if (lw < 2)
      ring_loader<8, K1_NST, 3, 6, 2>([&](int kt, int buf) {
#pragma unroll
        for (int i = 0; i < 3; ++i) glds16(x + kt * 64, avoff[i], lb + buf * K1_ST + (ap0 + i) * 1024);
      }, ldB);
    else
      ring_loader<8, K1_NST, 2, 6, 2>([&](int kt, int buf) {
#pragma unroll
        for (int i = 0; i < 2; ++i) glds16(x + kt * 64, avoff[i], lb + buf * K1_ST + (ap0 + i) * 1024);
      }, ldB);
    for (int i = blockIdx.x * 256 + (tid - 256); i < p.zero_words; i += 256 * 256) p.zero_area[i] = 0u;
    bar();
  } else {
    if (wid == 0 && dep0 != nullptr) { dep_wait(dep0); if (dep1 != nullptr) dep_wait(dep1); }
    f32x4 acc[14], cs[3];
#pragma unroll
    for (int i = 0; i < 14; ++i) acc[i] = (f32x4){0.f, 0.f, 0.f, 0.f};
#pragma unroll
    for (int i = 0; i < 3; ++i) cs[i] = (f32x4){0.f, 0.f, 0.f, 0.f};
    const bf16x8 ones = {0x3F80, 0x3F80, 0x3F80, 0x3F80, 0x3F80, 0x3F80, 0x3F80, 0x3F80};
    int aoff[2];
#pragma unroll
    for (int ks = 0; ks < 2; ++ks) aoff[ks] = l15 * 128 + (((4 * ks + g4) ^ (l15 & 7)) << 4);
    const int fl = ((lane >> 3) & 1) | (((lane >> 4) & 1) << 1);
    const int boff = K1_AST + (8 * g4 + (l15 >> 2)) * 128 + 8 * (lane & 3) + ((hw ^ fl) << 5);
    f32x4 sm = {0.f, 0.f, 0.f, 0.f}, sg = sm, sm4 = sm, sg4 = sm;
    bf16x8 af[2][5], bf[2][3], sf[2];
    auto rdfr = [&](int ks, int buf) {
      const char* sb = smem + buf * K1_ST;
#pragma unroll
      for (int pn = 0; pn < 3; ++pn) bf[ks][pn] = lds_tr2(sb + boff + pn * 8192 + ks * 4096);
#pragma unroll
      for (int mt = 0; mt < 5; ++mt) af[ks][mt] = lds_b128(sb + mt * 2048 + aoff[ks]);
      sf[ks] = lds_b128(sb + hw * 2048 + aoff[ks]);
    };
    auto mm = [&](int ks) {
#pragma unroll
      for (int pn = 0; pn < 3; ++pn) cs[pn] = mfma(bf[ks][pn], ones, cs[pn]);
#pragma unroll
      for (int mt = 0; mt < 5; ++mt) {
        if (mt > 0) acc[mt - 1] = mfma(bf[ks][0], af[ks][mt], acc[mt - 1]);
        acc[4 + mt] = mfma(bf[ks][1], af[ks][mt], acc[4 + mt]);
        acc[9 + mt] = mfma(bf[ks][2], af[ks][mt], acc[9 + mt]);
      }
      sm = mfma(ones, sf[ks], sm); sg = mfma(sf[ks], sf[ks], sg);
      sm4 = mfma(ones, af[ks][4], sm4); sg4 = mfma(af[ks][4], af[ks][4], sg4);
    };
    ring_consumer<8, K1_NST, 12, 21>(rdfr, mm);
    {
      const f32x2 st = stat_fin(sm, sg, lane), st4 = stat_fin(sm4, sg4, lane);
      if (lane < 16) { sst[hw * 16 + lane] = st; if (hw == 0) sst[64 + lane] = st4; }
    }
    bar();
    char* sw = smem + hw * HSZ;
    char* sq = sw; char* sk = sw + 2 * QH; char* sv = sk + 2 * KH;
    const int half = g4 >> 1, o8 = (g4 & 1) * 8;
#pragma unroll
    for (int mt = 0; mt < 5; ++mt) {
      const f32x2 ms = sst[mt * 16 + l15];
      auto put = [&](char* base, int hs, int row, const f32x4& a, const f32x4& c) {
        u32x2 pq = {pack2((a[0] - ms[0] * c[0]) * ms[1], (a[1] - ms[0] * c[1]) * ms[1]),
                    pack2((a[2] - ms[0] * c[2]) * ms[1], (a[3] - ms[0] * c[3]) * ms[1])};
        *(u32x2*)(base + half * hs + row * 16 + o8) = pq;
      };
      if (mt > 0) put(sq, QH, (mt - 1) * 16 + l15, acc[mt - 1], cs[0]);
      put(sk, KH, mt * 16 + l15, acc[4 + mt], cs[1]);
      put(sv, KH, mt * 16 + l15, acc[9 + mt], cs[2]);
    }
  }
  bar();
  {
    const int hf = lane & 1, tl = mh * 32 + (lane >> 1);
    const int t = r0 + tl;
    const char* sq = smem + hw * HSZ + hf * QH;
    const char* sk = smem + hw * HSZ + 2 * QH + hf * KH;
    const char* sv = sk + 2 * KH;
    u32x4 q0 = *(const u32x4*)(sq + tl * 16);
    float lg[8];
    float mx = -3.0e38f;
#pragma unroll
    for (int j = 0; j < 8; ++j) {
      u32x4 k0 = *(const u32x4*)(sk + (tl + 16 - 2 * j) * 16);
      float d = 0.f;
#pragma unroll
      for (int e = 0; e < 4; ++e) d = dot2(q0[e], k0[e], d);
      d += dpp_f<0xB1>(d);
      d = rbf(d) * 0.25f;
      d = (t - 2 * j >= 0) ? d : -1.0e9f;
      lg[j] = d;
      mx = fmaxf(mx, d);
    }
    float sum = 0.f;
#pragma unroll
    for (int j = 0; j < 8; ++j) { lg[j] = __expf(lg[j] - mx); sum += lg[j]; }
    const float rs = 1.0f / sum;
    float ov[8];
#pragma unroll
    for (int e = 0; e < 8; ++e) ov[e] = 0.f;
#pragma unroll
    for (int jp = 0; jp < 4; ++jp) {
      const u32 pp = pack2(lg[2 * jp] * rs, lg[2 * jp + 1] * rs);
      int ea = tl + 16 - 4 * jp;
      u32x4 va = *(const u32x4*)(sv + ea * 16), vb = *(const u32x4*)(sv + (ea - 2) * 16);
#pragma unroll
      for (int e = 0; e < 4; ++e) {
        u32 lo = __builtin_amdgcn_perm(vb[e], va[e], 0x05040100u);
        u32 hi = __builtin_amdgcn_perm(vb[e], va[e], 0x07060302u);
        ov[2 * e] = dot2(lo, pp, ov[2 * e]);
        ov[2 * e + 1] = dot2(hi, pp, ov[2 * e + 1]);
      }
    }
    u32x4 w0;
#pragma unroll
    for (int e = 0; e < 4; ++e) w0[e] = pack2(ov[2 * e], ov[2 * e + 1]);
    st16(p.o + (size_t)t * D_ + hg * 64 + hw * 16 + hf * 8, w0);
  }
  publish(sig);
}

template <int LDA, int NT, int KW>
DEVI void phase_proj(char* smem, const Ctx& c, const u16* __restrict__ A, const u16* __restrict__ W,
                     const u16* __restrict__ res, u16* __restrict__ out, const u32* dep, u32* sig) {
  constexpr int ST = 16384, NST = 8;
  const int lane = c.lane, wid = c.wid;
  const int r0 = c.rt * 64, c0 = c.c8 * 64;
  const u32 lb = c.lb;
  if (wid >= 4) {
    const int lw = wid - 4;
    const u32 avoff = (u32)((lane >> 3) * (LDA * 2) + (((lane & 7) ^ (lane >> 3)) << 4));
    ring_loader<NT, NST, 2, 2, 7>(
        [&](int kt, int buf) {
#pragma unroll
          for (int i = 0; i < 2; ++i)
            glds16(A + (size_t)(r0 + 8 * (lw * 2 + i)) * LDA + kt * 64, avoff, lb + buf * ST + (lw * 2 + i) * 1024);
        },
        [&](int kt, int buf) {
#pragma unroll
          for (int i = 0; i < 2; ++i) {
            int f = ((lane >> 4) & 1) | (i << 1);
            int sc = ((((lane & 7) >> 1) ^ f) << 1) | (lane & 1);
            int krow = kt * 64 + 8 * (lw * 2 + i) + (lane >> 3);
            krow = krow < KW ? krow : KW - 1;
            glds16(W + c0, (u32)(krow * (D_ * 2) + sc * 16), lb + buf * ST + 8192 + (lw * 2 + i) * 1024);
          }
        });
  } else {
    const int l15 = lane & 15, g4 = lane >> 4;
    const int wm = wid >> 1, wn = wid & 1;
    u32x2 rv[2][2];
#pragma unroll
    for (int i = 0; i < 2; ++i)
#pragma unroll
      for (int j = 0; j < 2; ++j)
        rv[i][j] = *(const u32x2*)(res + (size_t)(r0 + wm * 32 + i * 16 + l15) * D_ + c0 + wn * 32 + j * 16 + 4 * g4);
    if (wid == 0 && dep != nullptr) dep_wait(dep);
    f32x4 acc[2][2];
#pragma unroll
    for (int i = 0; i < 2; ++i)
#pragma unroll
      for (int j = 0; j < 2; ++j) acc[i][j] = (f32x4){0.f, 0.f, 0.f, 0.f};
    int aoff[2], boff[2];
    const int fl = ((lane >> 3) & 1) | (((lane >> 4) & 1) << 1);
#pragma unroll
    for (int ks = 0; ks < 2; ++ks) aoff[ks] = (wm * 32 + l15) * 128 + (((4 * ks + g4) ^ (l15 & 7)) << 4);
#pragma unroll
    for (int j = 0; j < 2; ++j) boff[j] = 8192 + (8 * g4 + (l15 >> 2)) * 128 + 8 * (lane & 3) + (((wn * 2 + j) ^ fl) << 5);
    bf16x8 af[2][2], bf[2][2];
    ring_consumer<NT, NST, 6, 4>(
        [&](int ks, int buf) {
          const char* sb = smem + buf * ST;
#pragma unroll
          for (int i = 0; i < 2; ++i) af[ks][i] = lds_b128(sb + i * 2048 + aoff[ks]);
#pragma unroll
          for (int j = 0; j < 2; ++j) bf[ks][j] = lds_tr2(sb + boff[j] + ks * 4096);
        },
        [&](int ks) {
#pragma unroll
          for (int i = 0; i < 2; ++i)
#pragma unroll
            for (int j = 0; j < 2; ++j) acc[i][j] = mfma(bf[ks][j], af[ks][i], acc[i][j]);
        });
#pragma unroll
    for (int i = 0; i < 2; ++i)
#pragma unroll
      for (int j = 0; j < 2; ++j) {
        u32x2 r = rv[i][j], ov;
        ov[0] = pack2(bflo(r[0]) + rbf(acc[i][j][0]), bfhi(r[0]) + rbf(acc[i][j][1]));
        ov[1] = pack2(bflo(r[1]) + rbf(acc[i][j][2]), bfhi(r[1]) + rbf(acc[i][j][3]));
        st8(out + (size_t)(r0 + wm * 32 + i * 16 + l15) * D_ + c0 + wn * 32 + j * 16 + 4 * g4, ov);
      }
  }
  publish(sig);
}

constexpr int K3_AST = 64 * 128;
constexpr int K3_ST = K3_AST + 64 * 192 * 2;
constexpr int K3_NST = 4;
constexpr int K3E_ACC = 0;
constexpr int K3E_ST = 36864;
constexpr int K3E_FIN = 38912;
constexpr int K3E_OUT = 40960;

template <bool Z2>
DEVI void k3_epi(char* smem, f32x4 (&acc)[2][3], const f32x4 (&cs)[3], u16* __restrict__ z1, u16* __restrict__ z2t,
                 u64* __restrict__ gran, int rt, int ct, int lane, int wn, int mt0, int tid) {
  const int r0 = rt * 64;
  const int l15 = lane & 15, g4 = lane >> 4;
  const f32x2* sst = (const f32x2*)(smem + K3_NST * K3_ST);
  const int u0 = ct * 12 + wn * 3;
  int nv = 43 - u0; nv = nv < 0 ? 0 : (nv > 3 ? 3 : nv);
  const int ncols = (ct < 3 ? 12 : 7) * 16;
  u32 zp[2][3][2];
#pragma unroll
  for (int i = 0; i < 2; ++i) {
    f32x2 mn[2], rs[2];
    if constexpr (Z2) {
      const f32x4 a = *(const f32x4*)(sst + (mt0 + i) * 16 + 4 * g4), b = *(const f32x4*)(sst + (mt0 + i) * 16 + 4 * g4 + 2);
      mn[0] = (f32x2){a[0], a[2]}; rs[0] = (f32x2){a[1], a[3]};
      mn[1] = (f32x2){b[0], b[2]}; rs[1] = (f32x2){b[1], b[3]};
    } else {
      const f32x2 ms = sst[(mt0 + i) * 16 + l15];
      mn[0] = mn[1] = (f32x2){ms[0], ms[0]}; rs[0] = rs[1] = (f32x2){ms[1], ms[1]};
    }
#pragma unroll
    for (int j = 0; j < 3; ++j)
#pragma unroll
      for (int hh = 0; hh < 2; ++hh) {
        f32x2 a = {acc[i][j][2 * hh], acc[i][j][2 * hh + 1]}, c = {cs[j][2 * hh], cs[j][2 * hh + 1]};
        f32x2 y = (a - mn[hh] * c) * rs[hh];
        f32x2 g = gelu2(unpk(pack2(y[0], y[1])));
        zp[i][j][hh] = pack2(g[0], g[1]);
      }
  }
  if constexpr (!Z2) {
    char* img = smem + K3E_OUT;
#pragma unroll
    for (int i = 0; i < 2; ++i)
#pragma unroll
      for (int j = 0; j < 3; ++j)
        *(u32x2*)(img + ((mt0 + i) * 16 + l15) * 400 + (wn * 48 + j * 16 + 4 * g4) * 2) = (u32x2){zp[i][j][0], zp[i][j][1]};
    bar();
#pragma unroll
    for (int it = 0; it < 3; ++it) {
      int c = tid + it * 512, row = c / 24, ch = c - row * 24;
      if (ch * 8 < ncols)
        *(u32x4*)(z1 + (size_t)(r0 + row) * FH + ct * 192 + ch * 8) = *(const u32x4*)(img + row * 400 + ch * 16);
    }
  } else {
    f32x2* st = (f32x2*)(smem + K3E_ST);
    f32x2* fin = (f32x2*)(smem + K3E_FIN);
    float sv[16];
#pragma unroll
    for (int i = 0; i < 2; ++i)
#pragma unroll
      for (int hh = 0; hh < 2; ++hh) {
        f32x2 s = {0.f, 0.f}, q = {0.f, 0.f};
#pragma unroll
        for (int j = 0; j < 3; ++j) {
          const f32x2 z = unpk(j < nv ? zp[i][j][hh] : 0u);
          s += z; q += z * z;
        }
        sv[(i * 2 + hh) * 4 + 0] = s[0]; sv[(i * 2 + hh) * 4 + 1] = q[0];
        sv[(i * 2 + hh) * 4 + 2] = s[1]; sv[(i * 2 + hh) * 4 + 3] = q[1];
      }
#pragma unroll
    for (int k = 0; k < 16; ++k) sv[k] += dpp_f<0xB1>(sv[k]);
#pragma unroll
    for (int k = 0; k < 16; ++k) sv[k] += dpp_f<0x4E>(sv[k]);
#pragma unroll
    for (int k = 0; k < 16; ++k) sv[k] += dpp_f<0x141>(sv[k]);
#pragma unroll
    for (int k = 0; k < 16; ++k) sv[k] += dpp_f<0x140>(sv[k]);
    if (l15 == 0) {
#pragma unroll
      for (int i = 0; i < 2; ++i)
#pragma unroll
        for (int hh = 0; hh < 2; ++hh)
          *(f32x4*)(st + wn * 64 + (mt0 + i) * 16 + 4 * g4 + 2 * hh) =
              (f32x4){sv[(i * 2 + hh) * 4], sv[(i * 2 + hh) * 4 + 1], sv[(i * 2 + hh) * 4 + 2], sv[(i * 2 + hh) * 4 + 3]};
    }
    bar();
    if (tid < 64) {
      const f32x2 p = st[tid] + st[64 + tid] + st[128 + tid] + st[192 + tid];
      u64* gbase = gran + (size_t)(rt * 4) * 128;
      __hip_atomic_store(gbase + ct * 128 + tid * 2, (1ull << 32) | (u64)__float_as_uint(p[0]), __ATOMIC_RELAXED, __HIP_MEMORY_SCOPE_AGENT);
      __hip_atomic_store(gbase + ct * 128 + tid * 2 + 1, (1ull << 32) | (u64)__float_as_uint(p[1]), __ATOMIC_RELAXED, __HIP_MEMORY_SCOPE_AGENT);
      float ts, tq;
      int spins = 0;
      bool ok;
      do {
        ok = true; ts = 0.f; tq = 0.f;
#pragma unroll
        for (int c = 0; c < 4; ++c) {
          u64 a = __hip_atomic_load(gbase + c * 128 + tid * 2, __ATOMIC_RELAXED, __HIP_MEMORY_SCOPE_AGENT);
          u64 b = __hip_atomic_load(gbase + c * 128 + tid * 2 + 1, __ATOMIC_RELAXED, __HIP_MEMORY_SCOPE_AGENT);
          ok = ok && (a >> 32) == 1ull && (b >> 32) == 1ull;
          ts += __uint_as_float((u32)a); tq += __uint_as_float((u32)b);
        }
      } while (!ok && ++spins < (1 << 20));
      const float tmean = ts * (1.0f / 688.0f);
      const float vb = rbf(fmaxf(tq * (1.0f / 688.0f) - tmean * tmean, 0.f));
      const float sd = rbf(__builtin_sqrtf(rbf(vb + 1e-5f)));
      fin[tid] = (f32x2){rbf(tmean), 1.0f / sd};
    }
    bar();
    char* img = smem + K3E_OUT;
#pragma unroll
    for (int i = 0; i < 2; ++i) {
      const int row = (mt0 + i) * 16 + 4 * g4;
      const f32x4 a = *(const f32x4*)(fin + row), b = *(const f32x4*)(fin + row + 2);
      const f32x2 mn0 = {a[0], a[2]}, rs0 = {a[1], a[3]}, mn1 = {b[0], b[2]}, rs1 = {b[1], b[3]};
#pragma unroll
      for (int j = 0; j < 3; ++j) {
        f32x2 d0 = unpk(zp[i][j][0]) - mn0, d1 = unpk(zp[i][j][1]) - mn1;
        d0 = unpk(pack2(d0[0], d0[1])) * rs0; d1 = unpk(pack2(d1[0], d1[1])) * rs1;
        *(u32x2*)(img + (wn * 48 + j * 16 + l15) * 144 + row * 2) = (u32x2){pack2(d0[0], d0[1]), pack2(d1[0], d1[1])};
      }
    }
    bar();
#pragma unroll
    for (int it = 0; it < 3; ++it) {
      int c = tid + it * 512, f = c >> 3, ch = c & 7;
      if (f < ncols)
        *(u32x4*)(z2t + (size_t)(ct * 192 + f) * S_ + r0 + ch * 8) = *(const u32x4*)(img + f * 144 + ch * 16);
    }
  }
}

template <bool Z2>
DEVI void k3_compute(char* smem, u16* __restrict__ z1, u16* __restrict__ z2t,
                     u64* __restrict__ gran, int rt, int ct, int lane, int wn, int tid) {
  f32x2* sst = (f32x2*)(smem + K3_NST * K3_ST);
  f32x4 acc[2][3], acc2[2][3], cs[3];
#pragma unroll
  for (int i = 0; i < 2; ++i)
#pragma unroll
    for (int j = 0; j < 3; ++j) { acc[i][j] = (f32x4){0.f, 0.f, 0.f, 0.f}; acc2[i][j] = acc[i][j]; }
#pragma unroll
  for (int j = 0; j < 3; ++j) cs[j] = (f32x4){0.f, 0.f, 0.f, 0.f};
  const bf16x8 ones = {0x3F80, 0x3F80, 0x3F80, 0x3F80, 0x3F80, 0x3F80, 0x3F80, 0x3F80};
  const int l15 = lane & 15, g4 = lane >> 4;
  int aoff[2], boff[3];
#pragma unroll
  for (int ks = 0; ks < 2; ++ks) aoff[ks] = l15 * 128 + (((4 * ks + g4) ^ (l15 & 7)) << 4);
  const int fl = ((lane >> 3) & 1) | (((lane >> 4) & 1) << 1);
#pragma unroll
  for (int j = 0; j < 3; ++j) {
    int nt = wn * 3 + j;
    boff[j] = K3_AST + (nt >> 2) * 8192 + (8 * g4 + (l15 >> 2)) * 128 + 8 * (lane & 3) + (((nt & 3) ^ fl) << 5);
  }
  f32x4 sm = {0.f, 0.f, 0.f, 0.f}, sg = sm;
  bf16x8 af[2][4], bf[2][3], sf[2];
  auto rdfr = [&](int ks, int buf) {
    const char* sb = smem + buf * K3_ST;
#pragma unroll
    for (int j = 0; j < 3; ++j) bf[ks][j] = lds_tr2(sb + boff[j] + ks * 4096);
#pragma unroll
    for (int mt = 0; mt < 4; ++mt) af[ks][mt] = lds_b128(sb + mt * 2048 + aoff[ks]);
    sf[ks] = lds_b128(sb + wn * 2048 + aoff[ks]);
  };
  auto mm = [&](int ks) {
#pragma unroll
    for (int j = 0; j < 3; ++j) {
      if constexpr (Z2) cs[j] = mfma(ones, bf[ks][j], cs[j]);
      else cs[j] = mfma(bf[ks][j], ones, cs[j]);
    }
#pragma unroll
    for (int mt = 0; mt < 2; ++mt)
#pragma unroll
      for (int j = 0; j < 3; ++j) {
        if constexpr (Z2) { acc[mt][j] = mfma(af[ks][mt], bf[ks][j], acc[mt][j]); acc2[mt][j] = mfma(af[ks][2 + mt], bf[ks][j], acc2[mt][j]); }
        else { acc[mt][j] = mfma(bf[ks][j], af[ks][mt], acc[mt][j]); acc2[mt][j] = mfma(bf[ks][j], af[ks][2 + mt], acc2[mt][j]); }
      }
    sm = mfma(ones, sf[ks], sm); sg = mfma(sf[ks], sf[ks], sg);
  };
  ring_consumer<8, K3_NST, 11, 17>(rdfr, mm);
  bar();
  {
    const f32x2 st = stat_fin(sm, sg, lane);
    if (lane < 16) sst[wn * 16 + lane] = st;
    f32x4* ho = (f32x4*)(smem + K3E_ACC) + wn * 9 * 64 + lane;
#pragma unroll
    for (int i = 0; i < 2; ++i)
#pragma unroll
      for (int j = 0; j < 3; ++j) ho[(i * 3 + j) * 64] = acc2[i][j];
#pragma unroll
    for (int j = 0; j < 3; ++j) ho[(6 + j) * 64] = cs[j];
  }
  bar();
  k3_epi<Z2>(smem, acc, cs, z1, z2t, gran, rt, ct, lane, wn, 0, tid);
}

template <bool Z2>
DEVI void k3_helper(char* smem, u16* __restrict__ z1, u16* __restrict__ z2t,
                    u64* __restrict__ gran, int rt, int ct, int lane, int wn, int tid) {
  bar();
  bar();
  f32x4 acc[2][3], cs[3];
  const f32x4* hi = (const f32x4*)(smem + K3E_ACC) + wn * 9 * 64 + lane;
#pragma unroll
  for (int i = 0; i < 2; ++i)
#pragma unroll
    for (int j = 0; j < 3; ++j) acc[i][j] = hi[(i * 3 + j) * 64];
#pragma unroll
  for (int j = 0; j < 3; ++j) cs[j] = hi[(6 + j) * 64];
  k3_epi<Z2>(smem, acc, cs, z1, z2t, gran, rt, ct, lane, wn, 2, tid);
}

DEVI void phase3(char* smem, const Params& p, const Ctx& c, const u32* dep) {
  const u16* __restrict__ x = p.x1;
  const u16* __restrict__ w = p.w_in;
  const int tid = c.tid, lane = c.lane, wid = c.wid, rt = c.rt, c8 = c.c8;
  const int r0 = rt * 64;
  const u32 lb = c.lb;
  if (wid >= 4) {
    const int lw = wid - 4;
    const int cbase = (c8 >> 2) * FH + (c8 & 3) * 192;
    u32 bvoff[6];
#pragma unroll
    for (int i = 0; i < 6; ++i) {
      int pi = lw * 6 + i, panel = pi >> 3, pp = pi & 7;
      int f = ((lane >> 4) & 1) | ((pp & 1) << 1);
      int sc = ((((lane & 7) >> 1) ^ f) << 1) | (lane & 1);
      int col = cbase + panel * 64 + sc * 8;
      col = col <= F2 - 8 ? col : F2 - 8;
      bvoff[i] = (u32)((lane >> 3) * (F2 * 2) + col * 2);
    }
    const u32 avoff = (u32)((lane >> 3) * (D_ * 2) + (((lane & 7) ^ (lane >> 3)) << 4));
    ring_loader<8, K3_NST, 2, 6, 2>(
        [&](int kt, int buf) {
#pragma unroll
          for (int i = 0; i < 2; ++i)
            glds16(x + (size_t)(r0 + 8 * (lw * 2 + i)) * D_ + kt * 64, avoff, lb + buf * K3_ST + (lw * 2 + i) * 1024);
        },
        [&](int kt, int buf) {
#pragma unroll
          for (int i = 0; i < 6; ++i) {
            int pi = lw * 6 + i, panel = pi >> 3, pp = pi & 7;
            glds16(w + (size_t)(kt * 64 + 8 * pp) * F2, bvoff[i], lb + buf * K3_ST + K3_AST + panel * 8192 + pp * 1024);
          }
        });
    if (c8 < 4) k3_helper<false>(smem, p.z1, p.z2t, p.gran, rt, c8, lane, lw, tid);
    else k3_helper<true>(smem, p.z1, p.z2t, p.gran, rt, c8 - 4, lane, lw, tid);
  } else {
    if (wid == 0 && dep != nullptr) dep_wait(dep);
    if (c8 < 4) k3_compute<false>(smem, p.z1, p.z2t, p.gran, rt, c8, lane, wid, tid);
    else k3_compute<true>(smem, p.z1, p.z2t, p.gran, rt, c8 - 4, lane, wid, tid);
  }
}

constexpr int K4_ST = (64 + 96) * 128;
constexpr int K4_NST = 8;
constexpr int K4_NT = 32;

DEVI void phase4(char* smem, const Params& p, const Ctx& c, u32* sig) {
  const int lane = c.lane, wid = c.wid, nt0 = c.c8;
  const int r0 = c.rt * 64;
  const int u0 = nt0 * 6 < 37 ? nt0 * 6 : 37;
  const int f0 = u0 * 16;
  if (wid >= 4) {
    const int lw = wid - 4;
    const u16* asrc = p.w_sp + (size_t)(r0 + 16 * lw) * S_;
    const u16* bsrc = p.z2t_in + (size_t)(f0 + 24 * lw) * S_;
    const u32 lb = c.lb;
    const u32 voff = (u32)((lane >> 3) * (S_ * 2) + (((lane & 7) ^ (lane >> 3)) << 4));
    ring_loader<K4_NT, K4_NST, 2, 3, K4_NST - 1>(
        [&](int kt, int buf) {
#pragma unroll
          for (int i = 0; i < 2; ++i) glds16(asrc + (size_t)(8 * i) * S_ + kt * 64, voff, lb + buf * K4_ST + (lw * 2 + i) * 1024);
        },
        [&](int kt, int buf) {
#pragma unroll
          for (int i = 0; i < 3; ++i) glds16(bsrc + (size_t)(8 * i) * S_ + kt * 64, voff, lb + buf * K4_ST + 8192 + (lw * 3 + i) * 1024);
        });
  } else {
    const int l15 = lane & 15, g4 = lane >> 4;
    const int wm = wid >> 1, wn = wid & 1;
    u32x2 zv[2][3];
    float bs[2];
#pragma unroll
    for (int i = 0; i < 2; ++i) {
      bs[i] = bf2f(p.b_sp[r0 + wm * 32 + i * 16 + l15]);
#pragma unroll
      for (int j = 0; j < 3; ++j)
        zv[i][j] = *(const u32x2*)(p.z1_in + (size_t)(r0 + wm * 32 + i * 16 + l15) * FH + f0 + wn * 48 + j * 16 + 4 * g4);
    }
    f32x4 acc[2][3];
#pragma unroll
    for (int i = 0; i < 2; ++i)
#pragma unroll
      for (int j = 0; j < 3; ++j) acc[i][j] = (f32x4){0.f, 0.f, 0.f, 0.f};
    int aoff[2], boff[2];
#pragma unroll
    for (int ks = 0; ks < 2; ++ks) {
      aoff[ks] = (wm * 32 + l15) * 128 + (((4 * ks + g4) ^ (l15 & 7)) << 4);
      boff[ks] = 8192 + (wn * 48 + l15) * 128 + (((4 * ks + g4) ^ (l15 & 7)) << 4);
    }
    bf16x8 af[2][2], bf[2][3];
    ring_consumer<K4_NT, K4_NST, 5, 6>(
        [&](int ks, int buf) {
          const char* sb = smem + buf * K4_ST;
#pragma unroll
          for (int i = 0; i < 2; ++i) af[ks][i] = lds_b128(sb + i * 2048 + aoff[ks]);
#pragma unroll
          for (int j = 0; j < 3; ++j) bf[ks][j] = lds_b128(sb + j * 2048 + boff[ks]);
        },
        [&](int ks) {
#pragma unroll
          for (int i = 0; i < 2; ++i)
#pragma unroll
            for (int j = 0; j < 3; ++j) acc[i][j] = mfma(bf[ks][j], af[ks][i], acc[i][j]);
        });
    const int umin = nt0 * 6;
#pragma unroll
    for (int i = 0; i < 2; ++i) {
      u16* hp = p.h + (size_t)(r0 + wm * 32 + i * 16 + l15) * FHP;
#pragma unroll
      for (int j = 0; j < 3; ++j) {
        int u = u0 + wn * 3 + j;
        if (u >= umin) {
          u32x2 z = zv[i][j], ov;
          float g0 = rbf(rbf(acc[i][j][0]) + bs[i]), g1 = rbf(rbf(acc[i][j][1]) + bs[i]);
          float g2 = rbf(rbf(acc[i][j][2]) + bs[i]), g3 = rbf(rbf(acc[i][j][3]) + bs[i]);
          ov[0] = pack2(bflo(z[0]) * g0, bfhi(z[0]) * g1);
          ov[1] = pack2(bflo(z[1]) * g2, bfhi(z[1]) * g3);
          st8(hp + u * 16 + 4 * g4, ov);
        }
      }
      if (nt0 == 7 && wn == 1) st8(hp + FH + 4 * g4, (u32x2){0u, 0u});
    }
  }
  publish(sig);
}

__global__ __launch_bounds__(512, 2) void fused_phases(const Params p) {
    __shared__ __attribute__((aligned(16))) char smem[163840];
  Ctx c;
  c.tid = threadIdx.x; c.lane = c.tid & 63;
  c.wid = __builtin_amdgcn_readfirstlane(c.tid >> 6);
  const int bid = blockIdx.x, xcd = bid & 7, idx = bid >> 3;
  c.rt = (xcd >> 1) * 8 + (idx & 7);
  c.c8 = (xcd & 1) * 4 + (idx >> 3);
  c.lb = lds_addr(smem);
  const int ph = p.phases;
  u32* cnt = p.cnt + c.rt * 32;
#define DS(b)
  GSTAMP(lid, 0);
  if (ph & 1) { DS(6); phase4(smem, p, c, cnt); GSTAMP(lid, 1); }
  if (ph & 2) { DS(9); phase_proj<FHP, 11, FH>(smem, c, p.h, p.w_out, p.x1_in, p.xout, cnt, (ph & 4) ? cnt + 1024 : nullptr); GSTAMP(lid, 2); }
  if (ph & 4) {
    DS(12);
    phase1(smem, p, c, (ph & 2) ? cnt + 1024 : nullptr, ((ph & 2) && c.rt > 0) ? cnt + 1024 - 32 : nullptr,
           (ph & 8) ? cnt + 2048 : nullptr);
    GSTAMP(lid, 3);
  }
  if (ph & 8) { DS(15); phase_proj<D_, 8, D_>(smem, c, p.o, p.wo, p.xin, p.x1, (ph & 4) ? cnt + 2048 : nullptr, cnt + 3072); GSTAMP(lid, 4); }
  if (ph & 16) { DS(18); phase3(smem, p, c, cnt + 3072); GSTAMP(lid, 5); }
}

extern "C" void kernel_launch(void* const* d_in, const int* in_sizes, int n_in, void* d_out, int out_size,
                              void* d_ws, size_t ws_size, hipStream_t stream) {
  char* ws = (char*)d_ws;
  constexpr size_t MB = 1 << 20;
  u64* gran = (u64*)ws;
  u32* cnt = (u32*)(ws + 256 * 1024);
  const int zero_words = (256 * 1024 + 3 * 16384) / 4;
  auto buf = [&](int i) { return (u16*)(ws + (1 + 3 * i) * MB); };
  u16 *o[2] = {buf(0), buf(1)}, *x1[2] = {buf(2), buf(3)}, *z1[2] = {buf(4), buf(5)}, *z2t[2] = {buf(6), buf(7)};
  u16 *h[2] = {buf(8), buf(9)}, *x2 = buf(10);

  Params p;
  p.wqkv = (const u16*)d_in[1]; p.wo = (const u16*)d_in[2]; p.w_in = (const u16*)d_in[4];
  p.w_sp = (const u16*)d_in[5]; p.b_sp = (const u16*)d_in[6]; p.w_out = (const u16*)d_in[7];
  p.zero_area = (u32*)ws;
  p.z2t_in = z2t[0]; p.z1_in = z1[0]; p.x1_in = x1[0]; p.h = h[0]; p.xout = x2;
  p.xin = (const u16*)d_in[0]; p.o = o[0]; p.x1 = x1[0]; p.z1 = z1[0]; p.z2t = z2t[0]; p.gran = gran;
  p.cnt = cnt; p.zero_words = zero_words; p.phases = 4;
  fused_phases<<<256, 512, 0, stream>>>(p);
  p.zero_words = 0; p.phases = 24;
  fused_phases<<<256, 512, 0, stream>>>(p);
  p.xin = x2; p.o = o[1]; p.x1 = x1[1]; p.z1 = z1[1]; p.z2t = z2t[1]; p.gran = gran + 16384;
  p.cnt = cnt + 4096; p.phases = 31;
  fused_phases<<<256, 512, 0, stream>>>(p);
  p.z2t_in = z2t[1]; p.z1_in = z1[1]; p.x1_in = x1[1]; p.h = h[1]; p.xout = (u16*)d_out;
  p.cnt = cnt + 8192; p.phases = 3;
  fused_phases<<<256, 512, 0, stream>>>(p);
}
```

```cpp
#include <hip/hip_runtime.h>
#include <hip/hip_bf16.h>
#include <stdint.h>

typedef __attribute__((ext_vector_type(8))) short bf16x8;
typedef __attribute__((ext_vector_type(4))) short s16x4;
typedef __attribute__((ext_vector_type(4))) float f32x4;
typedef __attribute__((ext_vector_type(2))) float f32x2;
typedef __attribute__((ext_vector_type(2))) __bf16 bf16v2;
typedef __attribute__((ext_vector_type(4))) unsigned u32x4;
typedef __attribute__((ext_vector_type(2))) unsigned u32x2;
typedef unsigned short u16;
typedef unsigned int u32;
typedef unsigned long long u64;

#define LDS_AS __attribute__((address_space(3)))
#define GLB_AS __attribute__((address_space(1)))
#define DEVI __device__ __forceinline__

constexpr int S_ = 2048, D_ = 512, FH = 688, FHP = 704, F2 = 1376;

DEVI float bf2f(u32 v) { return __uint_as_float(v << 16); }
DEVI float bflo(u32 w) { return __uint_as_float(w << 16); }
DEVI float bfhi(u32 w) { return __uint_as_float(w & 0xffff0000u); }
DEVI u32 pack2(float a, float b) {
  bf16v2 v; v[0] = (__bf16)a; v[1] = (__bf16)b;
  return __builtin_bit_cast(u32, v);
}
DEVI float dot2(u32 a, u32 b, float c) {
  return __builtin_amdgcn_fdot2_f32_bf16(__builtin_bit_cast(bf16v2, a), __builtin_bit_cast(bf16v2, b), c, false);
}
DEVI float rbf(float f) { return bf2f((u32)__builtin_bit_cast(u16, (__bf16)f)); }

DEVI void glds16(const void* sbase_, u32 voff, u32 lds_) {
  u32 keep;
  const u64 sb_ = (u64)sbase_;
  const u64 sbase = ((u64)(u32)__builtin_amdgcn_readfirstlane((int)(sb_ >> 32)) << 32) |
                    (u64)(u32)__builtin_amdgcn_readfirstlane((int)sb_);
  const u32 lds = (u32)__builtin_amdgcn_readfirstlane((int)lds_);
  asm volatile("s_mov_b32 %0, m0\n\ts_mov_b32 m0, %3\n\ts_nop 0\n\tglobal_load_lds_dwordx4 %1, %2\n\ts_mov_b32 m0, %0"
               : "=&s"(keep) : "v"(voff), "s"(sbase), "s"(lds) : "memory");
}
DEVI u32 lds_addr(const void* p) { return (u32)(size_t)(LDS_AS const char*)p; }
template <int N> DEVI void wait_vm() { asm volatile("s_waitcnt vmcnt(%0)" ::"n"(N) : "memory"); }
#define GSTAMP(k, i)
#define GS2(i)
DEVI void wait_lgkm0() {
  __builtin_amdgcn_sched_barrier(0);
  __builtin_amdgcn_s_waitcnt(0xC07F);
  asm volatile("" ::: "memory");
  __builtin_amdgcn_sched_barrier(0);
}
DEVI void bar() { wait_lgkm0(); __builtin_amdgcn_s_barrier(); __builtin_amdgcn_sched_barrier(0); }

DEVI void st8(void* p, u32x2 v) {
  __hip_atomic_store((u64*)p, ((u64)v[1] << 32) | (u64)v[0], __ATOMIC_RELAXED, __HIP_MEMORY_SCOPE_AGENT);
}
DEVI void st16(void* p, u32x4 v) {
  asm volatile("global_store_dwordx4 %0, %1, off sc1\n\ts_nop 1" ::"v"(p), "v"(v) : "memory");
}
DEVI void publish(u32* cnt, bool stored) {
  if (stored) wait_vm<0>();
  bar();
  if (cnt != nullptr && threadIdx.x == 0) __hip_atomic_fetch_add(cnt, 1u, __ATOMIC_RELAXED, __HIP_MEMORY_SCOPE_AGENT);
}
DEVI void dep_wait(const u32* cnt) {
  int spins = 0;
  while (__hip_atomic_load(cnt, __ATOMIC_RELAXED, __HIP_MEMORY_SCOPE_AGENT) < 8u && ++spins < (1 << 21))
    __builtin_amdgcn_s_sleep(1);
}

template <int I> struct IC { static constexpr int v = I; };
template <int N, int I = 0, class F> DEVI void sfor(F&& f) {
  if constexpr (I < N) { f(IC<I>{}); sfor<N, I + 1>(f); }
}

template <int NRD, int NMM> DEVI void interleave() {
  constexpr int NP = NRD < NMM ? NRD : NMM;
#pragma unroll
  for (int i = 0; i < NP; ++i) {
    __builtin_amdgcn_sched_group_barrier(0x008, 1, 0);
    __builtin_amdgcn_sched_group_barrier(0x100, 1, 0);
  }
  if constexpr (NMM > NP) __builtin_amdgcn_sched_group_barrier(0x008, NMM - NP, 0);
  if constexpr (NRD > NP) __builtin_amdgcn_sched_group_barrier(0x100, NRD - NP, 0);
}
template <int NT, int NST, int NRD, int NMM, class Rd, class Mm> DEVI void ring_consumer(Rd&& rdfr, Mm&& mm) {
  bar();
  GS2(0);
  bar();
  GS2(1);
  rdfr(0, 0);
  int rd = 0;
#pragma unroll 1
  for (int kt = 0; kt < NT - 1; ++kt) {
    wait_lgkm0();
    rdfr(1, rd);
    mm(0);
    interleave<NRD, NMM>();
    rd = rd + 1 == NST ? 0 : rd + 1;
    bar();
    rdfr(0, rd);
    mm(1);
    interleave<NRD, NMM>();
  }
  wait_lgkm0();
  rdfr(1, rd);
  mm(0);
  interleave<NRD, NMM>();
  wait_lgkm0();
  mm(1);
  GS2(2);
}
template <int NT, int NST, int LA, int LB, int PRE, class LdA, class LdB>
DEVI void ring_loader(bool pre_done, LdA&& ldA, LdB&& ldB) {
  static_assert(PRE + 1 <= NST && PRE + 1 <= NT, "ring");
  constexpr int LPW = LA + LB;
  if (!pre_done) {
#pragma unroll 1
    for (int i = 0; i < PRE; ++i) ldB(i, i);
  }
  bar();
#pragma unroll 1
  for (int i = 0; i < PRE; ++i) ldA(i, i);
  wait_vm<LA * (PRE - 1)>();
  bar();
  ldA(PRE, PRE); ldB(PRE, PRE);
  int wr = PRE + 1 == NST ? 0 : PRE + 1;
  sfor<PRE - 1>([&](auto ic) {
    constexpr int kt = decltype(ic)::v;
    constexpr int nfull = (kt + 1 < NT - PRE ? kt + 1 : NT - PRE);
    wait_vm<LA * (PRE - 2 - kt) + LPW * nfull>();
    bar();
    if constexpr (kt + PRE + 1 < NT) {
      ldA(kt + PRE + 1, wr); ldB(kt + PRE + 1, wr);
      wr = wr + 1 == NST ? 0 : wr + 1;
    }
  });
#pragma unroll 1
  for (int kt = PRE - 1; kt < NT - 1 - PRE; ++kt) {
    wait_vm<LPW * (PRE - 1)>();
    bar();
    ldA(kt + PRE + 1, wr); ldB(kt + PRE + 1, wr);
    wr = wr + 1 == NST ? 0 : wr + 1;
  }
  constexpr int K0 = (PRE - 1 > NT - 1 - PRE) ? PRE - 1 : NT - 1 - PRE;
  sfor<NT - 1 - K0>([&](auto ic) {
    constexpr int kt = K0 + decltype(ic)::v;
    wait_vm<LPW * (NT - 2 - kt)>();
    bar();
  });
}

template <int CTRL> DEVI float dpp_f(float x) {
  return __builtin_bit_cast(float, __builtin_amdgcn_update_dpp(0, __builtin_bit_cast(int, x), CTRL, 0xF, 0xF, true));
}
DEVI float row16_sum(float x) {
  x += dpp_f<0xB1>(x);
  x += dpp_f<0x4E>(x);
  x += dpp_f<0x141>(x);
  x += dpp_f<0x140>(x);
  return x;
}

DEVI bf16x8 lds_b128(const char* p) { return *(const bf16x8*)p; }
DEVI bf16x8 lds_tr2(const char* p) {
  s16x4 a = __builtin_amdgcn_ds_read_tr16_b64_v4i16((LDS_AS s16x4*)p);
  s16x4 b = __builtin_amdgcn_ds_read_tr16_b64_v4i16((LDS_AS s16x4*)(p + 512));
  return __builtin_shufflevector(a, b, 0, 1, 2, 3, 4, 5, 6, 7);
}
DEVI f32x4 mfma(bf16x8 a, bf16x8 b, f32x4 c) { return __builtin_amdgcn_mfma_f32_16x16x32_bf16(a, b, c, 0, 0, 0); }

DEVI f32x2 gelu2(f32x2 x) {
  const f32x2 c1 = {-2.3022081985f, -2.3022081985f};
  const f32x2 c2 = {-0.10294324f, -0.10294324f};
  const f32x2 one = {1.0f, 1.0f};
  f32x2 a = x * (x * x * c2 + c1);
  f32x2 e = {__builtin_amdgcn_exp2f(a[0]), __builtin_amdgcn_exp2f(a[1])};
  f32x2 d = e + one;
  f32x2 r = {__builtin_amdgcn_rcpf(d[0]), __builtin_amdgcn_rcpf(d[1])};
  return x * r;
}
DEVI f32x2 unpk(u32 w) { return (f32x2){bflo(w), bfhi(w)}; }

DEVI f32x2 stat_fin(const f32x4& sm, const f32x4& sg, int lane) {
  const int j = lane & 3;
  float dg = j == 0 ? sg[0] : (j == 1 ? sg[1] : (j == 2 ? sg[2] : sg[3]));
  const float q = __shfl(dg, (lane & 15) + 16 * ((lane & 15) >> 2));
  const float mean = sm[0] * (1.0f / 512.0f);
  const float var = fmaxf(q * (1.0f / 512.0f) - mean * mean, 0.f);
  const float sd = rbf(__builtin_sqrtf(rbf(rbf(var) + 1e-5f)));
  return (f32x2){rbf(mean), 1.0f / sd};
}

struct Params {
  const u16 *wqkv, *wo, *w_in, *w_sp, *b_sp, *w_out;
  const u16 *z2t_in, *z1_in, *x1_in;
  u16 *h, *xout;
  const u16* xin;
  u16 *o, *x1, *z1, *z2t;
  u64* gran;
  u32* cnt;
  u32* zero_area;
  int zero_words, phases;
};
struct Ctx { int tid, lane, wid, rt, c8; u32 lb; };

constexpr int K1_AST = 80 * 128;
constexpr int K1_ST = K1_AST + 64 * 192 * 2;
constexpr int K1_NST = 4;
constexpr int K3_AST = 64 * 128;
constexpr int K3_ST = K3_AST + 64 * 192 * 2;
constexpr int K3_NST = 4;
constexpr int PJ_ST = 16384, PJ_NST = 7, PJ_PRE = 6, PJ_BASE = 40960;

DEVI void p1_ldB(const Params& p, const Ctx& c, int kt, int buf) {
  const int lw = c.wid - 4, lane = c.lane;
#pragma unroll
  for (int i = 0; i < 6; ++i) {
    int pi = lw * 6 + i, panel = pi >> 3, pp = pi & 7;
    int f = ((lane >> 4) & 1) | ((i & 1) << 1);
    int sc = ((((lane & 7) >> 1) ^ f) << 1) | (lane & 1);
    glds16(p.wqkv + (size_t)(kt * 64 + 8 * pp) * 1536 + panel * 512 + c.c8 * 64, (u32)((lane >> 3) * 3072 + sc * 16),
           c.lb + buf * K1_ST + K1_AST + panel * 8192 + pp * 1024);
  }
}
DEVI void p3_ldB(const Params& p, const Ctx& c, int kt, int buf) {
  const int lw = c.wid - 4, lane = c.lane;
  const int cbase = (c.c8 >> 2) * FH + (c.c8 & 3) * 192;
#pragma unroll
  for (int i = 0; i < 6; ++i) {
    int pi = lw * 6 + i, panel = pi >> 3, pp = pi & 7;
    int f = ((lane >> 4) & 1) | ((i & 1) << 1);
    int sc = ((((lane & 7) >> 1) ^ f) << 1) | (lane & 1);
    int col = cbase + panel * 64 + sc * 8;
    col = col <= F2 - 8 ? col : F2 - 8;
    glds16(p.w_in + (size_t)(kt * 64 + 8 * pp) * F2, (u32)((lane >> 3) * (F2 * 2) + col * 2),
           c.lb + buf * K3_ST + K3_AST + panel * 8192 + pp * 1024);
  }
}
DEVI void pj_ldB(const u16* W, int KW, const Ctx& c, int kt, int buf) {
  const int lw = c.wid - 4, lane = c.lane;
#pragma unroll
  for (int i = 0; i < 2; ++i) {
    int f = ((lane >> 4) & 1) | (i << 1);
    int sc = ((((lane & 7) >> 1) ^ f) << 1) | (lane & 1);
    int krow = kt * 64 + 8 * (lw * 2 + i) + (lane >> 3);
    krow = krow < KW ? krow : KW - 1;
    glds16(W + c.c8 * 64, (u32)(krow * (D_ * 2) + sc * 16), c.lb + PJ_BASE + buf * PJ_ST + 8192 + (lw * 2 + i) * 1024);
  }
}
DEVI void next_pre(int next, const Params& p, const Ctx& c) {
  if (next == 5) {
#pragma unroll 1
    for (int i = 0; i < PJ_PRE; ++i) pj_ldB(p.w_out, FH, c, i, i);
  } else if (next == 2) {
#pragma unroll 1
    for (int i = 0; i < PJ_PRE; ++i) pj_ldB(p.wo, D_, c, i, i);
  } else if (next == 1) {
    p1_ldB(p, c, 0, 0); p1_ldB(p, c, 1, 1);
  } else if (next == 3) {
    p3_ldB(p, c, 0, 0); p3_ldB(p, c, 1, 1);
  }
}

DEVI void phase1(char* smem, const Params& p, const Ctx& c, const u32* dep0, const u32* dep1, u32* sig,
                 bool pre_done, int next) {
  f32x2* sst = (f32x2*)(smem + 31744);
  const u16* __restrict__ x = p.xin;
  const int tid = c.tid, lane = c.lane, wid = c.wid, hg = c.c8;
  const int r0 = c.rt * 64;
  const u32 lb = c.lb;
  const int hw = wid & 3, mh = wid >> 2;
  const int l15 = lane & 15, g4 = lane >> 4;
  constexpr int QH = 1152, KH = 1408, HSZ = 2 * QH + 4 * KH;
  if (wid >= 4) {
    const int lw = wid - 4;
    const int ap0 = lw < 2 ? lw * 3 : 6 + (lw - 2) * 2;
    u32 avoff[3];
#pragma unroll
    for (int i = 0; i < 3; ++i) {
      int gr = r0 - 16 + 8 * (ap0 + i) + (lane >> 3); gr = gr < 0 ? 0 : (gr > S_ - 1 ? S_ - 1 : gr);
      avoff[i] = (u32)(gr * (D_ * 2) + (((lane & 7) ^ (lane >> 3)) << 4));
    }
    auto ldB = [&](int kt, int buf) { p1_ldB(p, c, kt, buf); };
    if (lw < 2)
      ring_loader<8, K1_NST, 3, 6, 2>(pre_done, [&](int kt, int buf) {
#pragma unroll
        for (int i = 0; i < 3; ++i) glds16(x + kt * 64, avoff[i], lb + buf * K1_ST + (ap0 + i) * 1024);
      }, ldB);
    else
      ring_loader<8, K1_NST, 2, 6, 2>(pre_done, [&](int kt, int buf) {
#pragma unroll
        for (int i = 0; i < 2; ++i) glds16(x + kt * 64, avoff[i], lb + buf * K1_ST + (ap0 + i) * 1024);
      }, ldB);
    for (int i = blockIdx.x * 256 + (tid - 256); i < p.zero_words; i += 256 * 256) p.zero_area[i] = 0u;
    bar();
    next_pre(next, p, c);
    if (p.zero_words != 0) {
      const u32 junk = lb + 40960 + lw * 1024;
      const u32 vo = (u32)(lane * 16);
      const int b = blockIdx.x;
#pragma unroll 1
      for (int i = lw; i < 32; i += 4) glds16((const char*)p.w_sp + (size_t)b * 32768 + i * 1024, vo, junk);
      if (lw < 2) glds16((const char*)p.wo + (size_t)b * 2048 + lw * 1024, vo, junk);
      else if (b < 172) {
        glds16((const char*)p.w_out + (size_t)b * 4096 + (lw - 2) * 2048, vo, junk);
        glds16((const char*)p.w_out + (size_t)b * 4096 + (lw - 2) * 2048 + 1024, vo, junk);
      }
      if (b < 172) {
        glds16((const char*)p.w_in + (size_t)b * 8192 + lw * 2048, vo, junk);
        glds16((const char*)p.w_in + (size_t)b * 8192 + lw * 2048 + 1024, vo, junk);
      }
    }
    bar();
  } else {
    if (wid == 0 && dep0 != nullptr) { dep_wait(dep0); if (dep1 != nullptr) dep_wait(dep1); }
    f32x4 acc[14], cs[3];
#pragma unroll
    for (int i = 0; i < 14; ++i) acc[i] = (f32x4){0.f, 0.f, 0.f, 0.f};
#pragma unroll
    for (int i = 0; i < 3; ++i) cs[i] = (f32x4){0.f, 0.f, 0.f, 0.f};
    const bf16x8 ones = {0x3F80, 0x3F80, 0x3F80, 0x3F80, 0x3F80, 0x3F80, 0x3F80, 0x3F80};
    int aoff[2];
#pragma unroll
    for (int ks = 0; ks < 2; ++ks) aoff[ks] = l15 * 128 + (((4 * ks + g4) ^ (l15 & 7)) << 4);
    const int fl = ((lane >> 3) & 1) | (((lane >> 4) & 1) << 1);
    const int boff = K1_AST + (8 * g4 + (l15 >> 2)) * 128 + 8 * (lane & 3) + ((hw ^ fl) << 5);
    f32x4 sm = {0.f, 0.f, 0.f, 0.f}, sg = sm, sm4 = sm, sg4 = sm;
    bf16x8 af[2][5], bf[2][3], sf[2];
    auto rdfr = [&](int ks, int buf) {
      const char* sb = smem + buf * K1_ST;
#pragma unroll
      for (int pn = 0; pn < 3; ++pn) bf[ks][pn] = lds_tr2(sb + boff + pn * 8192 + ks * 4096);
#pragma unroll
      for (int mt = 0; mt < 5; ++mt) af[ks][mt] = lds_b128(sb + mt * 2048 + aoff[ks]);
      sf[ks] = lds_b128(sb + hw * 2048 + aoff[ks]);
    };
    auto mm = [&](int ks) {
#pragma unroll
      for (int pn = 0; pn < 3; ++pn) cs[pn] = mfma(bf[ks][pn], ones, cs[pn]);
#pragma unroll
      for (int mt = 0; mt < 5; ++mt) {
        if (mt > 0) acc[mt - 1] = mfma(bf[ks][0], af[ks][mt], acc[mt - 1]);
        acc[4 + mt] = mfma(bf[ks][1], af[ks][mt], acc[4 + mt]);
        acc[9 + mt] = mfma(bf[ks][2], af[ks][mt], acc[9 + mt]);
      }
      sm = mfma(ones, sf[ks], sm); sg = mfma(sf[ks], sf[ks], sg);
      sm4 = mfma(ones, af[ks][4], sm4); sg4 = mfma(af[ks][4], af[ks][4], sg4);
    };
    ring_consumer<8, K1_NST, 12, 21>(rdfr, mm);
    bar();
    {
      const f32x2 st = stat_fin(sm, sg, lane), st4 = stat_fin(sm4, sg4, lane);
      if (lane < 16) { sst[hw * 16 + lane] = st; if (hw == 0) sst[64 + lane] = st4; }
    }
    bar();
    char* sw = smem + hw * HSZ;
    char* sq = sw; char* sk = sw + 2 * QH; char* sv = sk + 2 * KH;
    const int half = g4 >> 1, o8 = (g4 & 1) * 8;
#pragma unroll
    for (int mt = 0; mt < 5; ++mt) {
      const f32x2 ms = sst[mt * 16 + l15];
      auto put = [&](char* base, int hs, int row, const f32x4& a, const f32x4& c) {
        u32x2 pq = {pack2((a[0] - ms[0] * c[0]) * ms[1], (a[1] - ms[0] * c[1]) * ms[1]),
                    pack2((a[2] - ms[0] * c[2]) * ms[1], (a[3] - ms[0] * c[3]) * ms[1])};
        *(u32x2*)(base + half * hs + row * 16 + o8) = pq;
      };
      if (mt > 0) put(sq, QH, (mt - 1) * 16 + l15, acc[mt - 1], cs[0]);
      put(sk, KH, mt * 16 + l15, acc[4 + mt], cs[1]);
      put(sv, KH, mt * 16 + l15, acc[9 + mt], cs[2]);
    }
  }
  bar();
  {
    const int hf = lane & 1, tl = mh * 32 + (lane >> 1);
    const int t = r0 + tl;
    const char* sq = smem + hw * HSZ + hf * QH;
    const char* sk = smem + hw * HSZ + 2 * QH + hf * KH;
    const char* sv = sk + 2 * KH;
    u32x4 q0 = *(const u32x4*)(sq + tl * 16);
    float lg[8];
    float mx = -3.0e38f;
#pragma unroll
    for (int j = 0; j < 8; ++j) {
      u32x4 k0 = *(const u32x4*)(sk + (tl + 16 - 2 * j) * 16);
      float d = 0.f;
#pragma unroll
      for (int e = 0; e < 4; ++e) d = dot2(q0[e], k0[e], d);
      d += dpp_f<0xB1>(d);
      d = rbf(d) * 0.25f;
      d = (t - 2 * j >= 0) ? d : -1.0e9f;
      lg[j] = d;
      mx = fmaxf(mx, d);
    }
    float sum = 0.f;
#pragma unroll
    for (int j = 0; j < 8; ++j) { lg[j] = __expf(lg[j] - mx); sum += lg[j]; }
    const float rs = 1.0f / sum;
    float ov[8];
#pragma unroll
    for (int e = 0; e < 8; ++e) ov[e] = 0.f;
#pragma unroll
    for (int jp = 0; jp < 4; ++jp) {
      const u32 pp = pack2(lg[2 * jp] * rs, lg[2 * jp + 1] * rs);
      int ea = tl + 16 - 4 * jp;
      u32x4 va = *(const u32x4*)(sv + ea * 16), vb = *(const u32x4*)(sv + (ea - 2) * 16);
#pragma unroll
      for (int e = 0; e < 4; ++e) {
        u32 lo = __builtin_amdgcn_perm(vb[e], va[e], 0x05040100u);
        u32 hi = __builtin_amdgcn_perm(vb[e], va[e], 0x07060302u);
        ov[2 * e] = dot2(lo, pp, ov[2 * e]);
        ov[2 * e + 1] = dot2(hi, pp, ov[2 * e + 1]);
      }
    }
    u32x4 w0;
#pragma unroll
    for (int e = 0; e < 4; ++e) w0[e] = pack2(ov[2 * e], ov[2 * e + 1]);
    st16(p.o + (size_t)t * D_ + hg * 64 + hw * 16 + hf * 8, w0);
  }
  publish(sig, true);
}

template <int LDA, int NT, int KW>
DEVI void phase_proj(char* smem, const Ctx& c, const u16* __restrict__ A, const u16* __restrict__ W,
                     const u16* __restrict__ res, u16* __restrict__ out, const u32* dep, u32* sig,
                     const Params& p, bool pre_done, int next) {
  constexpr int ST = PJ_ST, NST = PJ_NST;
  const int lane = c.lane, wid = c.wid;
  const int r0 = c.rt * 64, c0 = c.c8 * 64;
  const u32 lb = c.lb;
  if (wid >= 4) {
    const int lw = wid - 4;
    const u32 avoff = (u32)((lane >> 3) * (LDA * 2) + (((lane & 7) ^ (lane >> 3)) << 4));
    ring_loader<NT, NST, 2, 2, PJ_PRE>(
        pre_done,
        [&](int kt, int buf) {
#pragma unroll
          for (int i = 0; i < 2; ++i)
            glds16(A + (size_t)(r0 + 8 * (lw * 2 + i)) * LDA + kt * 64, avoff, lb + PJ_BASE + buf * ST + (lw * 2 + i) * 1024);
        },
        [&](int kt, int buf) { pj_ldB(W, KW, c, kt, buf); });
    bar();
    next_pre(next, p, c);
  } else {
    const int l15 = lane & 15, g4 = lane >> 4;
    const int wm = wid >> 1, wn = wid & 1;
    u32x2 rv[2][2];
#pragma unroll
    for (int i = 0; i < 2; ++i)
#pragma unroll
      for (int j = 0; j < 2; ++j)
        rv[i][j] = *(const u32x2*)(res + (size_t)(r0 + wm * 32 + i * 16 + l15) * D_ + c0 + wn * 32 + j * 16 + 4 * g4);
    if (wid == 0 && dep != nullptr) dep_wait(dep);
    f32x4 acc[2][2];
#pragma unroll
    for (int i = 0; i < 2; ++i)
#pragma unroll
      for (int j = 0; j < 2; ++j) acc[i][j] = (f32x4){0.f, 0.f, 0.f, 0.f};
    int aoff[2], boff[2];
    const int fl = ((lane >> 3) & 1) | (((lane >> 4) & 1) << 1);
#pragma unroll
    for (int ks = 0; ks < 2; ++ks) aoff[ks] = (wm * 32 + l15) * 128 + (((4 * ks + g4) ^ (l15 & 7)) << 4);
#pragma unroll
    for (int j = 0; j < 2; ++j) boff[j] = 8192 + (8 * g4 + (l15 >> 2)) * 128 + 8 * (lane & 3) + (((wn * 2 + j) ^ fl) << 5);
    bf16x8 af[2][2], bf[2][2];
    ring_consumer<NT, NST, 6, 4>(
        [&](int ks, int buf) {
          const char* sb = smem + PJ_BASE + buf * ST;
#pragma unroll
          for (int i = 0; i < 2; ++i) af[ks][i] = lds_b128(sb + i * 2048 + aoff[ks]);
#pragma unroll
          for (int j = 0; j < 2; ++j) bf[ks][j] = lds_tr2(sb + boff[j] + ks * 4096);
        },
        [&](int ks) {
#pragma unroll
          for (int i = 0; i < 2; ++i)
#pragma unroll
            for (int j = 0; j < 2; ++j) acc[i][j] = mfma(bf[ks][j], af[ks][i], acc[i][j]);
        });
    bar();
#pragma unroll
    for (int i = 0; i < 2; ++i)
#pragma unroll
      for (int j = 0; j < 2; ++j) {
        u32x2 r = rv[i][j], ov;
        ov[0] = pack2(bflo(r[0]) + rbf(acc[i][j][0]), bfhi(r[0]) + rbf(acc[i][j][1]));
        ov[1] = pack2(bflo(r[1]) + rbf(acc[i][j][2]), bfhi(r[1]) + rbf(acc[i][j][3]));
        st8(out + (size_t)(r0 + wm * 32 + i * 16 + l15) * D_ + c0 + wn * 32 + j * 16 + 4 * g4, ov);
      }
  }
  publish(sig, wid < 4);
}

constexpr int K3E_ACC = 0;
constexpr int K3E_ST = 36864;
constexpr int K3E_FIN = 38912;
constexpr int K3E_OUT = 40960;

template <bool Z2>
DEVI void k3_epi(char* smem, f32x4 (&acc)[2][3], const f32x4 (&cs)[3], u16* __restrict__ z1, u16* __restrict__ z2t,
                 u64* __restrict__ gran, int rt, int ct, int lane, int wn, int mt0, int tid) {
  const int r0 = rt * 64;
  const int l15 = lane & 15, g4 = lane >> 4;
  const f32x2* sst = (const f32x2*)(smem + K3_NST * K3_ST);
  const int u0 = ct * 12 + wn * 3;
  int nv = 43 - u0; nv = nv < 0 ? 0 : (nv > 3 ? 3 : nv);
  const int ncols = (ct < 3 ? 12 : 7) * 16;
  u32 zp[2][3][2];
#pragma unroll
  for (int i = 0; i < 2; ++i) {
    f32x2 mn[2], rs[2];
    if constexpr (Z2) {
      const f32x4 a = *(const f32x4*)(sst + (mt0 + i) * 16 + 4 * g4), b = *(const f32x4*)(sst + (mt0 + i) * 16 + 4 * g4 + 2);
      mn[0] = (f32x2){a[0], a[2]}; rs[0] = (f32x2){a[1], a[3]};
      mn[1] = (f32x2){b[0], b[2]}; rs[1] = (f32x2){b[1], b[3]};
    } else {
      const f32x2 ms = sst[(mt0 + i) * 16 + l15];
      mn[0] = mn[1] = (f32x2){ms[0], ms[0]}; rs[0] = rs[1] = (f32x2){ms[1], ms[1]};
    }
#pragma unroll
    for (int j = 0; j < 3; ++j)
#pragma unroll
      for (int hh = 0; hh < 2; ++hh) {
        f32x2 a = {acc[i][j][2 * hh], acc[i][j][2 * hh + 1]}, c = {cs[j][2 * hh], cs[j][2 * hh + 1]};
        f32x2 y = (a - mn[hh] * c) * rs[hh];
        f32x2 g = gelu2(unpk(pack2(y[0], y[1])));
        zp[i][j][hh] = pack2(g[0], g[1]);
      }
  }
  if constexpr (!Z2) {
    char* img = smem + K3E_OUT;
#pragma unroll
    for (int i = 0; i < 2; ++i)
#pragma unroll
      for (int j = 0; j < 3; ++j)
        *(u32x2*)(img + ((mt0 + i) * 16 + l15) * 400 + (wn * 48 + j * 16 + 4 * g4) * 2) = (u32x2){zp[i][j][0], zp[i][j][1]};
    bar();
#pragma unroll
    for (int it = 0; it < 3; ++it) {
      int c = tid + it * 512, row = c / 24, ch = c - row * 24;
      if (ch * 8 < ncols)
        *(u32x4*)(z1 + (size_t)(r0 + row) * FH + ct * 192 + ch * 8) = *(const u32x4*)(img + row * 400 + ch * 16);
    }
  } else {
    f32x2* st = (f32x2*)(smem + K3E_ST);
    f32x2* fin = (f32x2*)(smem + K3E_FIN);
    float sv[16];
#pragma unroll
    for (int i = 0; i < 2; ++i)
#pragma unroll
      for (int hh = 0; hh < 2; ++hh) {
        f32x2 s = {0.f, 0.f}, q = {0.f, 0.f};
#pragma unroll
        for (int j = 0; j < 3; ++j) {
          const f32x2 z = unpk(j < nv ? zp[i][j][hh] : 0u);
          s += z; q += z * z;
        }
        sv[(i * 2 + hh) * 4 + 0] = s[0]; sv[(i * 2 + hh) * 4 + 1] = q[0];
        sv[(i * 2 + hh) * 4 + 2] = s[1]; sv[(i * 2 + hh) * 4 + 3] = q[1];
      }
#pragma unroll
    for (int k = 0; k < 16; ++k) sv[k] += dpp_f<0xB1>(sv[k]);
#pragma unroll
    for (int k = 0; k < 16; ++k) sv[k] += dpp_f<0x4E>(sv[k]);
#pragma unroll
    for (int k = 0; k < 16; ++k) sv[k] += dpp_f<0x141>(sv[k]);
#pragma unroll
    for (int k = 0; k < 16; ++k) sv[k] += dpp_f<0x140>(sv[k]);
    if (l15 == 0) {
#pragma unroll
      for (int i = 0; i < 2; ++i)
#pragma unroll
        for (int hh = 0; hh < 2; ++hh)
          *(f32x4*)(st + wn * 64 + (mt0 + i) * 16 + 4 * g4 + 2 * hh) =
              (f32x4){sv[(i * 2 + hh) * 4], sv[(i * 2 + hh) * 4 + 1], sv[(i * 2 + hh) * 4 + 2], sv[(i * 2 + hh) * 4 + 3]};
    }
    bar();
    if (tid < 64) {
      const f32x2 p = st[tid] + st[64 + tid] + st[128 + tid] + st[192 + tid];
      u64* gbase = gran + (size_t)(rt * 4) * 128;
      __hip_atomic_store(gbase + ct * 128 + tid * 2, (1ull << 32) | (u64)__float_as_uint(p[0]), __ATOMIC_RELAXED, __HIP_MEMORY_SCOPE_AGENT);
      __hip_atomic_store(gbase + ct * 128 + tid * 2 + 1, (1ull << 32) | (u64)__float_as_uint(p[1]), __ATOMIC_RELAXED, __HIP_MEMORY_SCOPE_AGENT);
      float ts, tq;
      int spins = 0;
      bool ok;
      do {
        ok = true; ts = 0.f; tq = 0.f;
#pragma unroll
        for (int c = 0; c < 4; ++c) {
          u64 a = __hip_atomic_load(gbase + c * 128 + tid * 2, __ATOMIC_RELAXED, __HIP_MEMORY_SCOPE_AGENT);
          u64 b = __hip_atomic_load(gbase + c * 128 + tid * 2 + 1, __ATOMIC_RELAXED, __HIP_MEMORY_SCOPE_AGENT);
          ok = ok && (a >> 32) == 1ull && (b >> 32) == 1ull;
          ts += __uint_as_float((u32)a); tq += __uint_as_float((u32)b);
        }
      } while (!ok && ++spins < (1 << 20));
      const float tmean = ts * (1.0f / 688.0f);
      const float vb = rbf(fmaxf(tq * (1.0f / 688.0f) - tmean * tmean, 0.f));
      const float sd = rbf(__builtin_sqrtf(rbf(vb + 1e-5f)));
      fin[tid] = (f32x2){rbf(tmean), 1.0f / sd};
    }
    bar();
    char* img = smem + K3E_OUT;
#pragma unroll
    for (int i = 0; i < 2; ++i) {
      const int row = (mt0 + i) * 16 + 4 * g4;
      const f32x4 a = *(const f32x4*)(fin + row), b = *(const f32x4*)(fin + row + 2);
      const f32x2 mn0 = {a[0], a[2]}, rs0 = {a[1], a[3]}, mn1 = {b[0], b[2]}, rs1 = {b[1], b[3]};
#pragma unroll
      for (int j = 0; j < 3; ++j) {
        f32x2 d0 = unpk(zp[i][j][0]) - mn0, d1 = unpk(zp[i][j][1]) - mn1;
        d0 = unpk(pack2(d0[0], d0[1])) * rs0; d1 = unpk(pack2(d1[0], d1[1])) * rs1;
        *(u32x2*)(img + (wn * 48 + j * 16 + l15) * 144 + row * 2) = (u32x2){pack2(d0[0], d0[1]), pack2(d1[0], d1[1])};
      }
    }
    bar();
#pragma unroll
    for (int it = 0; it < 3; ++it) {
      int c = tid + it * 512, f = c >> 3, ch = c & 7;
      if (f < ncols)
        *(u32x4*)(z2t + (size_t)(ct * 192 + f) * S_ + r0 + ch * 8) = *(const u32x4*)(img + f * 144 + ch * 16);
    }
  }
}

template <bool Z2>
DEVI void k3_compute(char* smem, u16* __restrict__ z1, u16* __restrict__ z2t,
                     u64* __restrict__ gran, int rt, int ct, int lane, int wn, int tid) {
  f32x2* sst = (f32x2*)(smem + K3_NST * K3_ST);
  f32x4 acc[2][3], acc2[2][3], cs[3];
#pragma unroll
  for (int i = 0; i < 2; ++i)
#pragma unroll
    for (int j = 0; j < 3; ++j) { acc[i][j] = (f32x4){0.f, 0.f, 0.f, 0.f}; acc2[i][j] = acc[i][j]; }
#pragma unroll
  for (int j = 0; j < 3; ++j) cs[j] = (f32x4){0.f, 0.f, 0.f, 0.f};
  const bf16x8 ones = {0x3F80, 0x3F80, 0x3F80, 0x3F80, 0x3F80, 0x3F80, 0x3F80, 0x3F80};
  const int l15 = lane & 15, g4 = lane >> 4;
  int aoff[2], boff[3];
#pragma unroll
  for (int ks = 0; ks < 2; ++ks) aoff[ks] = l15 * 128 + (((4 * ks + g4) ^ (l15 & 7)) << 4);
  const int fl = ((lane >> 3) & 1) | (((lane >> 4) & 1) << 1);
#pragma unroll
  for (int j = 0; j < 3; ++j) {
    int nt = wn * 3 + j;
    boff[j] = K3_AST + (nt >> 2) * 8192 + (8 * g4 + (l15 >> 2)) * 128 + 8 * (lane & 3) + (((nt & 3) ^ fl) << 5);
  }
  f32x4 sm = {0.f, 0.f, 0.f, 0.f}, sg = sm;
  bf16x8 af[2][4], bf[2][3], sf[2];
  auto rdfr = [&](int ks, int buf) {
    const char* sb = smem + buf * K3_ST;
#pragma unroll
    for (int j = 0; j < 3; ++j) bf[ks][j] = lds_tr2(sb + boff[j] + ks * 4096);
#pragma unroll
    for (int mt = 0; mt < 4; ++mt) af[ks][mt] = lds_b128(sb + mt * 2048 + aoff[ks]);
    sf[ks] = lds_b128(sb + wn * 2048 + aoff[ks]);
  };
  auto mm = [&](int ks) {
#pragma unroll
    for (int j = 0; j < 3; ++j) {
      if constexpr (Z2) cs[j] = mfma(ones, bf[ks][j], cs[j]);
      else cs[j] = mfma(bf[ks][j], ones, cs[j]);
    }
#pragma unroll
    for (int mt = 0; mt < 2; ++mt)
#pragma unroll
      for (int j = 0; j < 3; ++j) {
        if constexpr (Z2) { acc[mt][j] = mfma(af[ks][mt], bf[ks][j], acc[mt][j]); acc2[mt][j] = mfma(af[ks][2 + mt], bf[ks][j], acc2[mt][j]); }
        else { acc[mt][j] = mfma(bf[ks][j], af[ks][mt], acc[mt][j]); acc2[mt][j] = mfma(bf[ks][j], af[ks][2 + mt], acc2[mt][j]); }
      }
    sm = mfma(ones, sf[ks], sm); sg = mfma(sf[ks], sf[ks], sg);
  };
  ring_consumer<8, K3_NST, 11, 17>(rdfr, mm);
  bar();
  {
    const f32x2 st = stat_fin(sm, sg, lane);
    if (lane < 16) sst[wn * 16 + lane] = st;
    f32x4* ho = (f32x4*)(smem + K3E_ACC) + wn * 9 * 64 + lane;
#pragma unroll
    for (int i = 0; i < 2; ++i)
#pragma unroll
      for (int j = 0; j < 3; ++j) ho[(i * 3 + j) * 64] = acc2[i][j];
#pragma unroll
    for (int j = 0; j < 3; ++j) ho[(6 + j) * 64] = cs[j];
  }
  bar();
  k3_epi<Z2>(smem, acc, cs, z1, z2t, gran, rt, ct, lane, wn, 0, tid);
}

template <bool Z2>
DEVI void k3_helper(char* smem, u16* __restrict__ z1, u16* __restrict__ z2t,
                    u64* __restrict__ gran, int rt, int ct, int lane, int wn, int tid) {
  bar();
  bar();
  f32x4 acc[2][3], cs[3];
  const f32x4* hi = (const f32x4*)(smem + K3E_ACC) + wn * 9 * 64 + lane;
#pragma unroll
  for (int i = 0; i < 2; ++i)
#pragma unroll
    for (int j = 0; j < 3; ++j) acc[i][j] = hi[(i * 3 + j) * 64];
#pragma unroll
  for (int j = 0; j < 3; ++j) cs[j] = hi[(6 + j) * 64];
  k3_epi<Z2>(smem, acc, cs, z1, z2t, gran, rt, ct, lane, wn, 2, tid);
}

DEVI void phase3(char* smem, const Params& p, const Ctx& c, const u32* dep, bool pre_done) {
  const u16* __restrict__ x = p.x1;
  const int tid = c.tid, lane = c.lane, wid = c.wid, rt = c.rt, c8 = c.c8;
  const int r0 = rt * 64;
  const u32 lb = c.lb;
  if (wid >= 4) {
    const int lw = wid - 4;
    const u32 avoff = (u32)((lane >> 3) * (D_ * 2) + (((lane & 7) ^ (lane >> 3)) << 4));
    ring_loader<8, K3_NST, 2, 6, 2>(
        pre_done,
        [&](int kt, int buf) {
#pragma unroll
          for (int i = 0; i < 2; ++i)
            glds16(x + (size_t)(r0 + 8 * (lw * 2 + i)) * D_ + kt * 64, avoff, lb + buf * K3_ST + (lw * 2 + i) * 1024);
        },
        [&](int kt, int buf) { p3_ldB(p, c, kt, buf); });
    if (c8 < 4) k3_helper<false>(smem, p.z1, p.z2t, p.gran, rt, c8, lane, lw, tid);
    else k3_helper<true>(smem, p.z1, p.z2t, p.gran, rt, c8 - 4, lane, lw, tid);
  } else {
    if (wid == 0 && dep != nullptr) dep_wait(dep);
    if (c8 < 4) k3_compute<false>(smem, p.z1, p.z2t, p.gran, rt, c8, lane, wid, tid);
    else k3_compute<true>(smem, p.z1, p.z2t, p.gran, rt, c8 - 4, lane, wid, tid);
  }
}

constexpr int K4_ST = (64 + 96) * 256;
constexpr int K4_NST = 4;
constexpr int K4_NT = 16;

DEVI void phase4(char* smem, const Params& p, const Ctx& c, u32* sig, int next, const Ctx& cn) {
  const int lane = c.lane, wid = c.wid, nt0 = c.c8;
  const int r0 = c.rt * 64;
  const int u0 = nt0 * 6 < 37 ? nt0 * 6 : 37;
  const int f0 = u0 * 16;
  if (wid >= 4) {
    const int lw = wid - 4;
    const u16* asrc = p.w_sp + (size_t)(r0 + 16 * lw) * S_;
    const u16* bsrc = p.z2t_in + (size_t)(f0 + 24 * lw) * S_;
    const u32 lb = c.lb;
    u32 voff[4];
#pragma unroll
    for (int i = 0; i < 4; ++i) voff[i] = (u32)((lane >> 4) * (S_ * 2) + (((lane & 15) ^ (4 * i + (lane >> 4))) << 4));
    const int bsh = (lw & 1) * 2;
    ring_loader<K4_NT, K4_NST, 4, 6, K4_NST - 1>(
        false,
        [&](int kt, int buf) {
#pragma unroll
          for (int i = 0; i < 4; ++i) glds16(asrc + (size_t)(4 * i) * S_ + kt * 128, voff[i], lb + buf * K4_ST + (lw * 4 + i) * 1024);
        },
        [&](int kt, int buf) {
#pragma unroll
          for (int i = 0; i < 6; ++i) {
            const u32 v = bsh ? voff[(i + 2) & 3] : voff[i & 3];
            glds16(bsrc + (size_t)(4 * i) * S_ + kt * 128, v, lb + buf * K4_ST + 16384 + (lw * 6 + i) * 1024);
          }
        });
    bar();
    next_pre(next, p, cn);
  } else {
    const int l15 = lane & 15, g4 = lane >> 4;
    const int wm = wid >> 1, wn = wid & 1;
    u32x2 zv[2][3];
    float bs[2];
#pragma unroll
    for (int i = 0; i < 2; ++i) {
      bs[i] = bf2f(p.b_sp[r0 + wm * 32 + i * 16 + l15]);
#pragma unroll
      for (int j = 0; j < 3; ++j)
        zv[i][j] = *(const u32x2*)(p.z1_in + (size_t)(r0 + wm * 32 + i * 16 + l15) * FH + f0 + wn * 48 + j * 16 + 4 * g4);
    }
    f32x4 acc[2][3];
#pragma unroll
    for (int i = 0; i < 2; ++i)
#pragma unroll
      for (int j = 0; j < 3; ++j) acc[i][j] = (f32x4){0.f, 0.f, 0.f, 0.f};
    int xo[4];
#pragma unroll
    for (int k4 = 0; k4 < 4; ++k4) xo[k4] = l15 * 256 + (((4 * k4 + g4) ^ l15) << 4);
    const int abase = wm * 32 * 256, bbase = 16384 + wn * 48 * 256;
    bf16x8 af[2][2][2], bf[2][2][3];
    ring_consumer<K4_NT, K4_NST, 10, 12>(
        [&](int hs, int buf) {
          const char* sb = smem + buf * K4_ST;
#pragma unroll
          for (int ks = 0; ks < 2; ++ks) {
#pragma unroll
            for (int i = 0; i < 2; ++i) af[hs][ks][i] = lds_b128(sb + abase + i * 4096 + xo[2 * hs + ks]);
#pragma unroll
            for (int j = 0; j < 3; ++j) bf[hs][ks][j] = lds_b128(sb + bbase + j * 4096 + xo[2 * hs + ks]);
          }
        },
        [&](int hs) {
#pragma unroll
          for (int ks = 0; ks < 2; ++ks)
#pragma unroll
            for (int i = 0; i < 2; ++i)
#pragma unroll
              for (int j = 0; j < 3; ++j) acc[i][j] = mfma(bf[hs][ks][j], af[hs][ks][i], acc[i][j]);
        });
    bar();
    const int umin = nt0 * 6;
#pragma unroll
    for (int i = 0; i < 2; ++i) {
      u16* hp = p.h + (size_t)(r0 + wm * 32 + i * 16 + l15) * FHP;
#pragma unroll
      for (int j = 0; j < 3; ++j) {
        int u = u0 + wn * 3 + j;
        if (u >= umin) {
          u32x2 z = zv[i][j], ov;
          float g0 = rbf(rbf(acc[i][j][0]) + bs[i]), g1 = rbf(rbf(acc[i][j][1]) + bs[i]);
          float g2 = rbf(rbf(acc[i][j][2]) + bs[i]), g3 = rbf(rbf(acc[i][j][3]) + bs[i]);
          ov[0] = pack2(bflo(z[0]) * g0, bfhi(z[0]) * g1);
          ov[1] = pack2(bflo(z[1]) * g2, bfhi(z[1]) * g3);
          st8(hp + u * 16 + 4 * g4, ov);
        }
      }
      if (nt0 == 7 && wn == 1) st8(hp + FH + 4 * g4, (u32x2){0u, 0u});
    }
  }
  publish(sig, wid < 4);
}

__global__ __launch_bounds__(512, 2) void fused_phases(const Params p) {
    __shared__ __attribute__((aligned(16))) char smem[163840];
  Ctx c;
  c.tid = threadIdx.x; c.lane = c.tid & 63;
  c.wid = __builtin_amdgcn_readfirstlane(c.tid >> 6);
  const int bid = blockIdx.x, xcd = bid & 7, idx = bid >> 3;
  c.rt = (xcd >> 1) * 8 + (idx & 7);
  c.c8 = (xcd & 1) * 4 + (idx >> 3);
  c.lb = lds_addr(smem);
  const int ph = p.phases;
  u32* cnt = p.cnt + c.rt * 32;
#define DS(b)
  GSTAMP(lid, 0);
#define P4MAP 0
  if (ph & 1) {
    DS(6);
    Ctx c4 = c;
    if (P4MAP == 1) { c4.rt = xcd * 4 + (idx & 3); c4.c8 = idx >> 2; }
    if (P4MAP == 2) { c4.rt = (xcd >> 2) * 16 + (idx & 15); c4.c8 = (xcd & 3) * 2 + (idx >> 4); }
    if (P4MAP == 3) { c4.rt = bid >> 3; c4.c8 = bid & 7; }
    phase4(smem, p, c4, p.cnt + c4.rt * 32, 5, c);
    GSTAMP(lid, 1);
  }
  if (ph & 2) { DS(9); phase_proj<FHP, 11, FH>(smem, c, p.h, p.w_out, p.x1_in, p.xout, cnt, (ph & 4) ? cnt + 1024 : nullptr, p, true, (ph & 4) ? 1 : 0); GSTAMP(lid, 2); }
  if (ph & 4) {
    DS(12);
    phase1(smem, p, c, (ph & 2) ? cnt + 1024 : nullptr, ((ph & 2) && c.rt > 0) ? cnt + 1024 - 32 : nullptr,
           (ph & 8) ? cnt + 2048 : nullptr, (ph & 2) != 0, (ph & 8) ? 2 : 0);
    GSTAMP(lid, 3);
  }
  if (ph & 8) { DS(15); phase_proj<D_, 8, D_>(smem, c, p.o, p.wo, p.xin, p.x1, (ph & 4) ? cnt + 2048 : nullptr, cnt + 3072, p, (ph & 4) != 0, 3); GSTAMP(lid, 4); }
  if (ph & 16) { DS(18); phase3(smem, p, c, cnt + 3072, true); GSTAMP(lid, 5); }
}

extern "C" void kernel_launch(void* const* d_in, const int* in_sizes, int n_in, void* d_out, int out_size,
                              void* d_ws, size_t ws_size, hipStream_t stream) {
  char* ws = (char*)d_ws;
  constexpr size_t MB = 1 << 20;
  u64* gran = (u64*)ws;
  u32* cnt = (u32*)(ws + 256 * 1024);
  const int zero_words = (256 * 1024 + 3 * 16384) / 4;
  auto buf = [&](int i) { return (u16*)(ws + (1 + 3 * i) * MB); };
  u16 *o[2] = {buf(0), buf(1)}, *x1[2] = {buf(2), buf(3)}, *z1[2] = {buf(4), buf(5)}, *z2t[2] = {buf(6), buf(7)};
  u16 *h[2] = {buf(8), buf(9)}, *x2 = buf(10);

  Params p;
  p.wqkv = (const u16*)d_in[1]; p.wo = (const u16*)d_in[2]; p.w_in = (const u16*)d_in[4];
  p.w_sp = (const u16*)d_in[5]; p.b_sp = (const u16*)d_in[6]; p.w_out = (const u16*)d_in[7];
  p.zero_area = (u32*)ws;
  p.z2t_in = z2t[0]; p.z1_in = z1[0]; p.x1_in = x1[0]; p.h = h[0]; p.xout = x2;
  p.xin = (const u16*)d_in[0]; p.o = o[0]; p.x1 = x1[0]; p.z1 = z1[0]; p.z2t = z2t[0]; p.gran = gran;
  p.cnt = cnt; p.zero_words = zero_words; p.phases = 4;
  fused_phases<<<256, 512, 0, stream>>>(p);
  p.zero_words = 0; p.phases = 24;
  fused_phases<<<256, 512, 0, stream>>>(p);
  p.xin = x2; p.o = o[1]; p.x1 = x1[1]; p.z1 = z1[1]; p.z2t = z2t[1]; p.gran = gran + 16384;
  p.cnt = cnt + 4096; p.phases = 31;
  fused_phases<<<256, 512, 0, stream>>>(p);
  p.z2t_in = z2t[1]; p.z1_in = z1[1]; p.x1_in = x1[1]; p.h = h[1]; p.xout = (u16*)d_out;
  p.cnt = cnt + 8192; p.phases = 3;
  fused_phases<<<256, 512, 0, stream>>>(p);
}
```

```cpp
#include <hip/hip_runtime.h>
#include <hip/hip_bf16.h>
#include <stdint.h>

typedef __attribute__((ext_vector_type(8))) short bf16x8;
typedef __attribute__((ext_vector_type(4))) short s16x4;
typedef __attribute__((ext_vector_type(4))) float f32x4;
typedef __attribute__((ext_vector_type(2))) float f32x2;
typedef __attribute__((ext_vector_type(2))) __bf16 bf16v2;
typedef __attribute__((ext_vector_type(4))) unsigned u32x4;
typedef __attribute__((ext_vector_type(2))) unsigned u32x2;
typedef unsigned short u16;
typedef unsigned int u32;
typedef unsigned long long u64;

#define LDS_AS __attribute__((address_space(3)))
#define GLB_AS __attribute__((address_space(1)))
#define DEVI __device__ __forceinline__

constexpr int S_ = 2048, D_ = 512, FH = 688, FHP = 704, F2 = 1376;

DEVI float bf2f(u32 v) { return __uint_as_float(v << 16); }
DEVI float bflo(u32 w) { return __uint_as_float(w << 16); }
DEVI float bfhi(u32 w) { return __uint_as_float(w & 0xffff0000u); }
DEVI u32 pack2(float a, float b) {
  bf16v2 v; v[0] = (__bf16)a; v[1] = (__bf16)b;
  return __builtin_bit_cast(u32, v);
}
DEVI float dot2(u32 a, u32 b, float c) {
  return __builtin_amdgcn_fdot2_f32_bf16(__builtin_bit_cast(bf16v2, a), __builtin_bit_cast(bf16v2, b), c, false);
}
DEVI float rbf(float f) { return bf2f((u32)__builtin_bit_cast(u16, (__bf16)f)); }

DEVI void glds16(const void* sbase_, u32 voff, u32 lds_) {
  u32 keep;
  const u64 sb_ = (u64)sbase_;
  const u64 sbase = ((u64)(u32)__builtin_amdgcn_readfirstlane((int)(sb_ >> 32)) << 32) |
                    (u64)(u32)__builtin_amdgcn_readfirstlane((int)sb_);
  const u32 lds = (u32)__builtin_amdgcn_readfirstlane((int)lds_);
  asm volatile("s_mov_b32 %0, m0\n\ts_mov_b32 m0, %3\n\ts_nop 0\n\tglobal_load_lds_dwordx4 %1, %2\n\ts_mov_b32 m0, %0"
               : "=&s"(keep) : "v"(voff), "s"(sbase), "s"(lds) : "memory");
}
DEVI u32 lds_addr(const void* p) { return (u32)(size_t)(LDS_AS const char*)p; }
template <int N> DEVI void wait_vm() { asm volatile("s_waitcnt vmcnt(%0)" ::"n"(N) : "memory"); }
#define GSTAMP(k, i)
#define GS2(i)
DEVI void wait_lgkm0() {
  __builtin_amdgcn_sched_barrier(0);
  __builtin_amdgcn_s_waitcnt(0xC07F);
  asm volatile("" ::: "memory");
  __builtin_amdgcn_sched_barrier(0);
}
DEVI void bar() { wait_lgkm0(); __builtin_amdgcn_s_barrier(); __builtin_amdgcn_sched_barrier(0); }

DEVI void st8(void* p, u32x2 v) {
  __hip_atomic_store((u64*)p, ((u64)v[1] << 32) | (u64)v[0], __ATOMIC_RELAXED, __HIP_MEMORY_SCOPE_AGENT);
}
DEVI void st16(void* p, u32x4 v) {
  asm volatile("global_store_dwordx4 %0, %1, off sc1\n\ts_nop 1" ::"v"(p), "v"(v) : "memory");
}
DEVI void publish(u32* cnt, bool stored) {
  if (stored) wait_vm<0>();
  bar();
  if (cnt != nullptr && threadIdx.x == 0) __hip_atomic_fetch_add(cnt, 1u, __ATOMIC_RELAXED, __HIP_MEMORY_SCOPE_AGENT);
}
DEVI void dep_wait(const u32* cnt) {
  int spins = 0;
  while (__hip_atomic_load(cnt, __ATOMIC_RELAXED, __HIP_MEMORY_SCOPE_AGENT) < 8u && ++spins < (1 << 21))
    __builtin_amdgcn_s_sleep(1);
}

template <int I> struct IC { static constexpr int v = I; };
template <int N, int I = 0, class F> DEVI void sfor(F&& f) {
  if constexpr (I < N) { f(IC<I>{}); sfor<N, I + 1>(f); }
}

template <int NRD, int NMM> DEVI void interleave() {
  constexpr int NP = NRD < NMM ? NRD : NMM;
#pragma unroll
  for (int i = 0; i < NP; ++i) {
    __builtin_amdgcn_sched_group_barrier(0x008, 1, 0);
    __builtin_amdgcn_sched_group_barrier(0x100, 1, 0);
  }
  if constexpr (NMM > NP) __builtin_amdgcn_sched_group_barrier(0x008, NMM - NP, 0);
  if constexpr (NRD > NP) __builtin_amdgcn_sched_group_barrier(0x100, NRD - NP, 0);
}
template <int NT, int NST, int NRD, int NMM, class Rd, class Mm> DEVI void ring_consumer(Rd&& rdfr, Mm&& mm) {
  bar();
  GS2(0);
  bar();
  GS2(1);
  rdfr(0, 0);
  int rd = 0;
#pragma unroll 1
  for (int kt = 0; kt < NT - 1; ++kt) {
    wait_lgkm0();
    rdfr(1, rd);
    mm(0);
    interleave<NRD, NMM>();
    rd = rd + 1 == NST ? 0 : rd + 1;
    bar();
    rdfr(0, rd);
    mm(1);
    interleave<NRD, NMM>();
  }
  wait_lgkm0();
  rdfr(1, rd);
  mm(0);
  interleave<NRD, NMM>();
  wait_lgkm0();
  mm(1);
  GS2(2);
}
template <int NT, int NST, int LA, int LB, int PRE, class LdA, class LdB>
DEVI void ring_loader(bool pre_done, LdA&& ldA, LdB&& ldB) {
  static_assert(PRE + 1 <= NST && PRE + 1 <= NT, "ring");
  constexpr int LPW = LA + LB;
  if (!pre_done) {
#pragma unroll 1
    for (int i = 0; i < PRE; ++i) ldB(i, i);
  }
  bar();
#pragma unroll 1
  for (int i = 0; i < PRE; ++i) ldA(i, i);
  wait_vm<LA * (PRE - 1)>();
  bar();
  ldA(PRE, PRE); ldB(PRE, PRE);
  int wr = PRE + 1 == NST ? 0 : PRE + 1;
  sfor<PRE - 1>([&](auto ic) {
    constexpr int kt = decltype(ic)::v;
    constexpr int nfull = (kt + 1 < NT - PRE ? kt + 1 : NT - PRE);
    wait_vm<LA * (PRE - 2 - kt) + LPW * nfull>();
    bar();
    if constexpr (kt + PRE + 1 < NT) {
      ldA(kt + PRE + 1, wr); ldB(kt + PRE + 1, wr);
      wr = wr + 1 == NST ? 0 : wr + 1;
    }
  });
#pragma unroll 1
  for (int kt = PRE - 1; kt < NT - 1 - PRE; ++kt) {
    wait_vm<LPW * (PRE - 1)>();
    bar();
    ldA(kt + PRE + 1, wr); ldB(kt + PRE + 1, wr);
    wr = wr + 1 == NST ? 0 : wr + 1;
  }
  constexpr int K0 = (PRE - 1 > NT - 1 - PRE) ? PRE - 1 : NT - 1 - PRE;
  sfor<NT - 1 - K0>([&](auto ic) {
    constexpr int kt = K0 + decltype(ic)::v;
    wait_vm<LPW * (NT - 2 - kt)>();
    bar();
  });
}

template <int CTRL> DEVI float dpp_f(float x) {
  return __builtin_bit_cast(float, __builtin_amdgcn_update_dpp(0, __builtin_bit_cast(int, x), CTRL, 0xF, 0xF, true));
}
DEVI float row16_sum(float x) {
  x += dpp_f<0xB1>(x);
  x += dpp_f<0x4E>(x);
  x += dpp_f<0x141>(x);
  x += dpp_f<0x140>(x);
  return x;
}

DEVI bf16x8 lds_b128(const char* p) { return *(const bf16x8*)p; }
DEVI bf16x8 lds_tr2(const char* p) {
  s16x4 a = __builtin_amdgcn_ds_read_tr16_b64_v4i16((LDS_AS s16x4*)p);
  s16x4 b = __builtin_amdgcn_ds_read_tr16_b64_v4i16((LDS_AS s16x4*)(p + 512));
  return __builtin_shufflevector(a, b, 0, 1, 2, 3, 4, 5, 6, 7);
}
DEVI f32x4 mfma(bf16x8 a, bf16x8 b, f32x4 c) { return __builtin_amdgcn_mfma_f32_16x16x32_bf16(a, b, c, 0, 0, 0); }

DEVI f32x2 gelu2(f32x2 x) {
  const f32x2 c1 = {-2.3022081985f, -2.3022081985f};
  const f32x2 c2 = {-0.10294324f, -0.10294324f};
  const f32x2 one = {1.0f, 1.0f};
  f32x2 a = x * (x * x * c2 + c1);
  f32x2 e = {__builtin_amdgcn_exp2f(a[0]), __builtin_amdgcn_exp2f(a[1])};
  f32x2 d = e + one;
  f32x2 r = {__builtin_amdgcn_rcpf(d[0]), __builtin_amdgcn_rcpf(d[1])};
  return x * r;
}
DEVI f32x2 unpk(u32 w) { return (f32x2){bflo(w), bfhi(w)}; }

DEVI f32x2 stat_fin(const f32x4& sm, const f32x4& sg, int lane) {
  const int j = lane & 3;
  float dg = j == 0 ? sg[0] : (j == 1 ? sg[1] : (j == 2 ? sg[2] : sg[3]));
  const float q = __shfl(dg, (lane & 15) + 16 * ((lane & 15) >> 2));
  const float mean = sm[0] * (1.0f / 512.0f);
  const float var = fmaxf(q * (1.0f / 512.0f) - mean * mean, 0.f);
  const float sd = rbf(__builtin_sqrtf(rbf(rbf(var) + 1e-5f)));
  return (f32x2){rbf(mean), 1.0f / sd};
}

struct Params {
  const u16 *wqkv, *wo, *w_in, *w_sp, *b_sp, *w_out;
  const u16 *z2t_in, *z1_in, *x1_in;
  u16 *h, *xout;
  const u16* xin;
  u16 *o, *x1, *z1, *z2t;
  u64* gran;
  u32* cnt;
  u32* zero_area;
  int zero_words, phases;
};
struct Ctx { int tid, lane, wid, rt, c8; u32 lb; };

constexpr int K1_AST = 80 * 128;
constexpr int K1_ST = K1_AST + 64 * 192 * 2;
constexpr int K1_NST = 4;
constexpr int K3_AST = 64 * 128;
constexpr int K3_ST = K3_AST + 64 * 192 * 2;
constexpr int K3_NST = 4;
constexpr int PJ_ST = 16384, PJ_NST = 7, PJ_PRE = 6, PJ_BASE = 40960;

DEVI void p1_ldB(const Params& p, const Ctx& c, int kt, int buf) {
  const int lw = c.wid - 4, lane = c.lane;
#pragma unroll
  for (int i = 0; i < 6; ++i) {
    int pi = lw * 6 + i, panel = pi >> 3, pp = pi & 7;
    int f = ((lane >> 4) & 1) | ((i & 1) << 1);
    int sc = ((((lane & 7) >> 1) ^ f) << 1) | (lane & 1);
    glds16(p.wqkv + (size_t)(kt * 64 + 8 * pp) * 1536 + panel * 512 + c.c8 * 64, (u32)((lane >> 3) * 3072 + sc * 16),
           c.lb + buf * K1_ST + K1_AST + panel * 8192 + pp * 1024);
  }
}
DEVI void p3_ldB(const Params& p, const Ctx& c, int kt, int buf) {
  const int lw = c.wid - 4, lane = c.lane;
  const int cbase = (c.c8 >> 2) * FH + (c.c8 & 3) * 192;
#pragma unroll
  for (int i = 0; i < 6; ++i) {
    int pi = lw * 6 + i, panel = pi >> 3, pp = pi & 7;
    int f = ((lane >> 4) & 1) | ((i & 1) << 1);
    int sc = ((((lane & 7) >> 1) ^ f) << 1) | (lane & 1);
    int col = cbase + panel * 64 + sc * 8;
    col = col <= F2 - 8 ? col : F2 - 8;
    glds16(p.w_in + (size_t)(kt * 64 + 8 * pp) * F2, (u32)((lane >> 3) * (F2 * 2) + col * 2),
           c.lb + buf * K3_ST + K3_AST + panel * 8192 + pp * 1024);
  }
}
DEVI void pj_ldB(const u16* W, int KW, const Ctx& c, int kt, int buf) {
  const int lw = c.wid - 4, lane = c.lane;
#pragma unroll
  for (int i = 0; i < 2; ++i) {
    int f = ((lane >> 4) & 1) | (i << 1);
    int sc = ((((lane & 7) >> 1) ^ f) << 1) | (lane & 1);
    int krow = kt * 64 + 8 * (lw * 2 + i) + (lane >> 3);
    krow = krow < KW ? krow : KW - 1;
    glds16(W + c.c8 * 64, (u32)(krow * (D_ * 2) + sc * 16), c.lb + PJ_BASE + buf * PJ_ST + 8192 + (lw * 2 + i) * 1024);
  }
}
DEVI void next_pre(int next, const Params& p, const Ctx& c) {
  if (next == 5) {
#pragma unroll 1
    for (int i = 0; i < PJ_PRE; ++i) pj_ldB(p.w_out, FH, c, i, i);
  } else if (next == 2) {
#pragma unroll 1
    for (int i = 0; i < PJ_PRE; ++i) pj_ldB(p.wo, D_, c, i, i);
  } else if (next == 1) {
    p1_ldB(p, c, 0, 0); p1_ldB(p, c, 1, 1);
  } else if (next == 3) {
    p3_ldB(p, c, 0, 0); p3_ldB(p, c, 1, 1);
  }
}

DEVI void phase1(char* smem, const Params& p, const Ctx& c, const u32* dep0, const u32* dep1, u32* sig,
                 bool pre_done, int next) {
  f32x2* sst = (f32x2*)(smem + 31744);
  const u16* __restrict__ x = p.xin;
  const int tid = c.tid, lane = c.lane, wid = c.wid, hg = c.c8;
  const int r0 = c.rt * 64;
  const u32 lb = c.lb;
  const int hw = wid & 3, mh = wid >> 2;
  const int l15 = lane & 15, g4 = lane >> 4;
  constexpr int QH = 1152, KH = 1408, HSZ = 2 * QH + 4 * KH;
  if (wid >= 4) {
    const int lw = wid - 4;
    const int ap0 = lw < 2 ? lw * 3 : 6 + (lw - 2) * 2;
    u32 avoff[3];
#pragma unroll
    for (int i = 0; i < 3; ++i) {
      int gr = r0 - 16 + 8 * (ap0 + i) + (lane >> 3); gr = gr < 0 ? 0 : (gr > S_ - 1 ? S_ - 1 : gr);
      avoff[i] = (u32)(gr * (D_ * 2) + (((lane & 7) ^ (lane >> 3)) << 4));
    }
    auto ldB = [&](int kt, int buf) { p1_ldB(p, c, kt, buf); };
    auto ldA3 = [&](int kt, int buf) {
#pragma unroll
      for (int i = 0; i < 3; ++i) glds16(x + kt * 64, avoff[i], lb + buf * K1_ST + (ap0 + i) * 1024);
    };
    auto ldA2 = [&](int kt, int buf) {
#pragma unroll
      for (int i = 0; i < 2; ++i) glds16(x + kt * 64, avoff[i], lb + buf * K1_ST + (ap0 + i) * 1024);
    };
    if (p.zero_words != 0) {
      const u32 junk = lb + K1_NST * K1_ST + lw * 1024;
      const u32 vo = (u32)(lane * 16);
      const int b = blockIdx.x, bc = b < 172 ? b : 171;
      auto ldBw = [&](int kt, int buf) {
        p1_ldB(p, c, kt, buf);
        glds16((const char*)p.w_sp + (size_t)b * 32768 + (kt * 4 + lw) * 1024, vo, junk);
        const char* e = kt < 2 ? (const char*)p.w_in + (size_t)bc * 8192 + (kt * 4 + lw) * 1024
                      : kt == 2 ? (const char*)p.w_out + (size_t)bc * 4096 + lw * 1024
                                : (const char*)p.wo + (size_t)b * 2048 + (lw & 1) * 1024;
        glds16(e, vo, junk);
      };
      if (lw < 2) ring_loader<8, K1_NST, 3, 8, 2>(false, ldA3, ldBw);
      else ring_loader<8, K1_NST, 2, 8, 2>(false, ldA2, ldBw);
    } else {
      if (lw < 2) ring_loader<8, K1_NST, 3, 6, 2>(pre_done, ldA3, ldB);
      else ring_loader<8, K1_NST, 2, 6, 2>(pre_done, ldA2, ldB);
    }
    for (int i = blockIdx.x * 256 + (tid - 256); i < p.zero_words; i += 256 * 256) p.zero_area[i] = 0u;
    bar();
    next_pre(next, p, c);
    bar();
  } else {
    if (wid == 0 && dep0 != nullptr) { dep_wait(dep0); if (dep1 != nullptr) dep_wait(dep1); }
    f32x4 acc[14], cs[3];
#pragma unroll
    for (int i = 0; i < 14; ++i) acc[i] = (f32x4){0.f, 0.f, 0.f, 0.f};
#pragma unroll
    for (int i = 0; i < 3; ++i) cs[i] = (f32x4){0.f, 0.f, 0.f, 0.f};
    const bf16x8 ones = {0x3F80, 0x3F80, 0x3F80, 0x3F80, 0x3F80, 0x3F80, 0x3F80, 0x3F80};
    int aoff[2];
#pragma unroll
    for (int ks = 0; ks < 2; ++ks) aoff[ks] = l15 * 128 + (((4 * ks + g4) ^ (l15 & 7)) << 4);
    const int fl = ((lane >> 3) & 1) | (((lane >> 4) & 1) << 1);
    const int boff = K1_AST + (8 * g4 + (l15 >> 2)) * 128 + 8 * (lane & 3) + ((hw ^ fl) << 5);
    f32x4 sm = {0.f, 0.f, 0.f, 0.f}, sg = sm, sm4 = sm, sg4 = sm;
    bf16x8 af[2][5], bf[2][3], sf[2];
    auto rdfr = [&](int ks, int buf) {
      const char* sb = smem + buf * K1_ST;
#pragma unroll
      for (int pn = 0; pn < 3; ++pn) bf[ks][pn] = lds_tr2(sb + boff + pn * 8192 + ks * 4096);
#pragma unroll
      for (int mt = 0; mt < 5; ++mt) af[ks][mt] = lds_b128(sb + mt * 2048 + aoff[ks]);
      sf[ks] = lds_b128(sb + hw * 2048 + aoff[ks]);
    };
    auto mm = [&](int ks) {
#pragma unroll
      for (int pn = 0; pn < 3; ++pn) cs[pn] = mfma(bf[ks][pn], ones, cs[pn]);
#pragma unroll
      for (int mt = 0; mt < 5; ++mt) {
        if (mt > 0) acc[mt - 1] = mfma(bf[ks][0], af[ks][mt], acc[mt - 1]);
        acc[4 + mt] = mfma(bf[ks][1], af[ks][mt], acc[4 + mt]);
        acc[9 + mt] = mfma(bf[ks][2], af[ks][mt], acc[9 + mt]);
      }
      sm = mfma(ones, sf[ks], sm); sg = mfma(sf[ks], sf[ks], sg);
      sm4 = mfma(ones, af[ks][4], sm4); sg4 = mfma(af[ks][4], af[ks][4], sg4);
    };
    ring_consumer<8, K1_NST, 12, 21>(rdfr, mm);
    bar();
    {
      const f32x2 st = stat_fin(sm, sg, lane), st4 = stat_fin(sm4, sg4, lane);
      if (lane < 16) { sst[hw * 16 + lane] = st; if (hw == 0) sst[64 + lane] = st4; }
    }
    bar();
    char* sw = smem + hw * HSZ;
    char* sq = sw; char* sk = sw + 2 * QH; char* sv = sk + 2 * KH;
    const int half = g4 >> 1, o8 = (g4 & 1) * 8;
#pragma unroll
    for (int mt = 0; mt < 5; ++mt) {
      const f32x2 ms = sst[mt * 16 + l15];
      auto put = [&](char* base, int hs, int row, const f32x4& a, const f32x4& c) {
        u32x2 pq = {pack2((a[0] - ms[0] * c[0]) * ms[1], (a[1] - ms[0] * c[1]) * ms[1]),
                    pack2((a[2] - ms[0] * c[2]) * ms[1], (a[3] - ms[0] * c[3]) * ms[1])};
        *(u32x2*)(base + half * hs + row * 16 + o8) = pq;
      };
      if (mt > 0) put(sq, QH, (mt - 1) * 16 + l15, acc[mt - 1], cs[0]);
      put(sk, KH, mt * 16 + l15, acc[4 + mt], cs[1]);
      put(sv, KH, mt * 16 + l15, acc[9 + mt], cs[2]);
    }
  }
  bar();
  {
    const int hf = lane & 1, tl = mh * 32 + (lane >> 1);
    const int t = r0 + tl;
    const char* sq = smem + hw * HSZ + hf * QH;
    const char* sk = smem + hw * HSZ + 2 * QH + hf * KH;
    const char* sv = sk + 2 * KH;
    u32x4 q0 = *(const u32x4*)(sq + tl * 16);
    float lg[8];
    float mx = -3.0e38f;
#pragma unroll
    for (int j = 0; j < 8; ++j) {
      u32x4 k0 = *(const u32x4*)(sk + (tl + 16 - 2 * j) * 16);
      float d = 0.f;
#pragma unroll
      for (int e = 0; e < 4; ++e) d = dot2(q0[e], k0[e], d);
      d += dpp_f<0xB1>(d);
      d = rbf(d) * 0.25f;
      d = (t - 2 * j >= 0) ? d : -1.0e9f;
      lg[j] = d;
      mx = fmaxf(mx, d);
    }
    float sum = 0.f;
#pragma unroll
    for (int j = 0; j < 8; ++j) { lg[j] = __expf(lg[j] - mx); sum += lg[j]; }
    const float rs = 1.0f / sum;
    float ov[8];
#pragma unroll
    for (int e = 0; e < 8; ++e) ov[e] = 0.f;
#pragma unroll
    for (int jp = 0; jp < 4; ++jp) {
      const u32 pp = pack2(lg[2 * jp] * rs, lg[2 * jp + 1] * rs);
      int ea = tl + 16 - 4 * jp;
      u32x4 va = *(const u32x4*)(sv + ea * 16), vb = *(const u32x4*)(sv + (ea - 2) * 16);
#pragma unroll
      for (int e = 0; e < 4; ++e) {
        u32 lo = __builtin_amdgcn_perm(vb[e], va[e], 0x05040100u);
        u32 hi = __builtin_amdgcn_perm(vb[e], va[e], 0x07060302u);
        ov[2 * e] = dot2(lo, pp, ov[2 * e]);
        ov[2 * e + 1] = dot2(hi, pp, ov[2 * e + 1]);
      }
    }
    u32x4 w0;
#pragma unroll
    for (int e = 0; e < 4; ++e) w0[e] = pack2(ov[2 * e], ov[2 * e + 1]);
    st16(p.o + (size_t)t * D_ + hg * 64 + hw * 16 + hf * 8, w0);
  }
  publish(sig, true);
}

template <int LDA, int NT, int KW>
DEVI void phase_proj(char* smem, const Ctx& c, const u16* __restrict__ A, const u16* __restrict__ W,
                     const u16* __restrict__ res, u16* __restrict__ out, const u32* dep, u32* sig,
                     const Params& p, bool pre_done, int next) {
  constexpr int ST = PJ_ST, NST = PJ_NST;
  const int lane = c.lane, wid = c.wid;
  const int r0 = c.rt * 64, c0 = c.c8 * 64;
  const u32 lb = c.lb;
  if (wid >= 4) {
    const int lw = wid - 4;
    const u32 avoff = (u32)((lane >> 3) * (LDA * 2) + (((lane & 7) ^ (lane >> 3)) << 4));
    ring_loader<NT, NST, 2, 2, PJ_PRE>(
        pre_done,
        [&](int kt, int buf) {
#pragma unroll
          for (int i = 0; i < 2; ++i)
            glds16(A + (size_t)(r0 + 8 * (lw * 2 + i)) * LDA + kt * 64, avoff, lb + PJ_BASE + buf * ST + (lw * 2 + i) * 1024);
        },
        [&](int kt, int buf) { pj_ldB(W, KW, c, kt, buf); });
    bar();
    next_pre(next, p, c);
  } else {
    const int l15 = lane & 15, g4 = lane >> 4;
    const int wm = wid >> 1, wn = wid & 1;
    u32x2 rv[2][2];
#pragma unroll
    for (int i = 0; i < 2; ++i)
#pragma unroll
      for (int j = 0; j < 2; ++j)
        rv[i][j] = *(const u32x2*)(res + (size_t)(r0 + wm * 32 + i * 16 + l15) * D_ + c0 + wn * 32 + j * 16 + 4 * g4);
    if (wid == 0 && dep != nullptr) dep_wait(dep);
    f32x4 acc[2][2];
#pragma unroll
    for (int i = 0; i < 2; ++i)
#pragma unroll
      for (int j = 0; j < 2; ++j) acc[i][j] = (f32x4){0.f, 0.f, 0.f, 0.f};
    int aoff[2], boff[2];
    const int fl = ((lane >> 3) & 1) | (((lane >> 4) & 1) << 1);
#pragma unroll
    for (int ks = 0; ks < 2; ++ks) aoff[ks] = (wm * 32 + l15) * 128 + (((4 * ks + g4) ^ (l15 & 7)) << 4);
#pragma unroll
    for (int j = 0; j < 2; ++j) boff[j] = 8192 + (8 * g4 + (l15 >> 2)) * 128 + 8 * (lane & 3) + (((wn * 2 + j) ^ fl) << 5);
    bf16x8 af[2][2], bf[2][2];
    ring_consumer<NT, NST, 6, 4>(
        [&](int ks, int buf) {
          const char* sb = smem + PJ_BASE + buf * ST;
#pragma unroll
          for (int i = 0; i < 2; ++i) af[ks][i] = lds_b128(sb + i * 2048 + aoff[ks]);
#pragma unroll
          for (int j = 0; j < 2; ++j) bf[ks][j] = lds_tr2(sb + boff[j] + ks * 4096);
        },
        [&](int ks) {
#pragma unroll
          for (int i = 0; i < 2; ++i)
#pragma unroll
            for (int j = 0; j < 2; ++j) acc[i][j] = mfma(bf[ks][j], af[ks][i], acc[i][j]);
        });
    bar();
#pragma unroll
    for (int i = 0; i < 2; ++i)
#pragma unroll
      for (int j = 0; j < 2; ++j) {
        u32x2 r = rv[i][j], ov;
        ov[0] = pack2(bflo(r[0]) + rbf(acc[i][j][0]), bfhi(r[0]) + rbf(acc[i][j][1]));
        ov[1] = pack2(bflo(r[1]) + rbf(acc[i][j][2]), bfhi(r[1]) + rbf(acc[i][j][3]));
        st8(out + (size_t)(r0 + wm * 32 + i * 16 + l15) * D_ + c0 + wn * 32 + j * 16 + 4 * g4, ov);
      }
  }
  publish(sig, wid < 4);
}

constexpr int K3E_ACC = 0;
constexpr int K3E_ST = 36864;
constexpr int K3E_FIN = 38912;
constexpr int K3E_OUT = 40960;

template <bool Z2>
DEVI void k3_epi(char* smem, f32x4 (&acc)[2][3], const f32x4 (&cs)[3], u16* __restrict__ z1, u16* __restrict__ z2t,
                 u64* __restrict__ gran, int rt, int ct, int lane, int wn, int mt0, int tid) {
  const int r0 = rt * 64;
  const int l15 = lane & 15, g4 = lane >> 4;
  const f32x2* sst = (const f32x2*)(smem + K3_NST * K3_ST);
  const int u0 = ct * 12 + wn * 3;
  int nv = 43 - u0; nv = nv < 0 ? 0 : (nv > 3 ? 3 : nv);
  const int ncols = (ct < 3 ? 12 : 7) * 16;
  u32 zp[2][3][2];
#pragma unroll
  for (int i = 0; i < 2; ++i) {
    f32x2 mn[2], rs[2];
    if constexpr (Z2) {
      const f32x4 a = *(const f32x4*)(sst + (mt0 + i) * 16 + 4 * g4), b = *(const f32x4*)(sst + (mt0 + i) * 16 + 4 * g4 + 2);
      mn[0] = (f32x2){a[0], a[2]}; rs[0] = (f32x2){a[1], a[3]};
      mn[1] = (f32x2){b[0], b[2]}; rs[1] = (f32x2){b[1], b[3]};
    } else {
      const f32x2 ms = sst[(mt0 + i) * 16 + l15];
      mn[0] = mn[1] = (f32x2){ms[0], ms[0]}; rs[0] = rs[1] = (f32x2){ms[1], ms[1]};
    }
#pragma unroll
    for (int j = 0; j < 3; ++j)
#pragma unroll
      for (int hh = 0; hh < 2; ++hh) {
        f32x2 a = {acc[i][j][2 * hh], acc[i][j][2 * hh + 1]}, c = {cs[j][2 * hh], cs[j][2 * hh + 1]};
        f32x2 y = (a - mn[hh] * c) * rs[hh];
        f32x2 g = gelu2(unpk(pack2(y[0], y[1])));
        zp[i][j][hh] = pack2(g[0], g[1]);
      }
  }
  if constexpr (!Z2) {
    char* img = smem + K3E_OUT;
#pragma unroll
    for (int i = 0; i < 2; ++i)
#pragma unroll
      for (int j = 0; j < 3; ++j)
        *(u32x2*)(img + ((mt0 + i) * 16 + l15) * 400 + (wn * 48 + j * 16 + 4 * g4) * 2) = (u32x2){zp[i][j][0], zp[i][j][1]};
    bar();
#pragma unroll
    for (int it = 0; it < 3; ++it) {
      int c = tid + it * 512, row = c / 24, ch = c - row * 24;
      if (ch * 8 < ncols)
        *(u32x4*)(z1 + (size_t)(r0 + row) * FH + ct * 192 + ch * 8) = *(const u32x4*)(img + row * 400 + ch * 16);
    }
  } else {
    f32x2* st = (f32x2*)(smem + K3E_ST);
    f32x2* fin = (f32x2*)(smem + K3E_FIN);
    float sv[16];
#pragma unroll
    for (int i = 0; i < 2; ++i)
#pragma unroll
      for (int hh = 0; hh < 2; ++hh) {
        f32x2 s = {0.f, 0.f}, q = {0.f, 0.f};
#pragma unroll
        for (int j = 0; j < 3; ++j) {
          const f32x2 z = unpk(j < nv ? zp[i][j][hh] : 0u);
          s += z; q += z * z;
        }
        sv[(i * 2 + hh) * 4 + 0] = s[0]; sv[(i * 2 + hh) * 4 + 1] = q[0];
        sv[(i * 2 + hh) * 4 + 2] = s[1]; sv[(i * 2 + hh) * 4 + 3] = q[1];
      }
#pragma unroll
    for (int k = 0; k < 16; ++k) sv[k] += dpp_f<0xB1>(sv[k]);
#pragma unroll
    for (int k = 0; k < 16; ++k) sv[k] += dpp_f<0x4E>(sv[k]);
#pragma unroll
    for (int k = 0; k < 16; ++k) sv[k] += dpp_f<0x141>(sv[k]);
#pragma unroll
    for (int k = 0; k < 16; ++k) sv[k] += dpp_f<0x140>(sv[k]);
    if (l15 == 0) {
#pragma unroll
      for (int i = 0; i < 2; ++i)
#pragma unroll
        for (int hh = 0; hh < 2; ++hh)
          *(f32x4*)(st + wn * 64 + (mt0 + i) * 16 + 4 * g4 + 2 * hh) =
              (f32x4){sv[(i * 2 + hh) * 4], sv[(i * 2 + hh) * 4 + 1], sv[(i * 2 + hh) * 4 + 2], sv[(i * 2 + hh) * 4 + 3]};
    }
    bar();
    if (tid < 64) {
      const f32x2 p = st[tid] + st[64 + tid] + st[128 + tid] + st[192 + tid];
      u64* gbase = gran + (size_t)(rt * 4) * 128;
      __hip_atomic_store(gbase + ct * 128 + tid * 2, (1ull << 32) | (u64)__float_as_uint(p[0]), __ATOMIC_RELAXED, __HIP_MEMORY_SCOPE_AGENT);
      __hip_atomic_store(gbase + ct * 128 + tid * 2 + 1, (1ull << 32) | (u64)__float_as_uint(p[1]), __ATOMIC_RELAXED, __HIP_MEMORY_SCOPE_AGENT);
      float ts, tq;
      int spins = 0;
      bool ok;
      do {
        ok = true; ts = 0.f; tq = 0.f;
#pragma unroll
        for (int c = 0; c < 4; ++c) {
          u64 a = __hip_atomic_load(gbase + c * 128 + tid * 2, __ATOMIC_RELAXED, __HIP_MEMORY_SCOPE_AGENT);
          u64 b = __hip_atomic_load(gbase + c * 128 + tid * 2 + 1, __ATOMIC_RELAXED, __HIP_MEMORY_SCOPE_AGENT);
          ok = ok && (a >> 32) == 1ull && (b >> 32) == 1ull;
          ts += __uint_as_float((u32)a); tq += __uint_as_float((u32)b);
        }
      } while (!ok && ++spins < (1 << 20));
      const float tmean = ts * (1.0f / 688.0f);
      const float vb = rbf(fmaxf(tq * (1.0f / 688.0f) - tmean * tmean, 0.f));
      const float sd = rbf(__builtin_sqrtf(rbf(vb + 1e-5f)));
      fin[tid] = (f32x2){rbf(tmean), 1.0f / sd};
    }
    bar();
    char* img = smem + K3E_OUT;
#pragma unroll
    for (int i = 0; i < 2; ++i) {
      const int row = (mt0 + i) * 16 + 4 * g4;
      const f32x4 a = *(const f32x4*)(fin + row), b = *(const f32x4*)(fin + row + 2);
      const f32x2 mn0 = {a[0], a[2]}, rs0 = {a[1], a[3]}, mn1 = {b[0], b[2]}, rs1 = {b[1], b[3]};
#pragma unroll
      for (int j = 0; j < 3; ++j) {
        f32x2 d0 = unpk(zp[i][j][0]) - mn0, d1 = unpk(zp[i][j][1]) - mn1;
        d0 = unpk(pack2(d0[0], d0[1])) * rs0; d1 = unpk(pack2(d1[0], d1[1])) * rs1;
        *(u32x2*)(img + (wn * 48 + j * 16 + l15) * 144 + row * 2) = (u32x2){pack2(d0[0], d0[1]), pack2(d1[0], d1[1])};
      }
    }
    bar();
#pragma unroll
    for (int it = 0; it < 3; ++it) {
      int c = tid + it * 512, f = c >> 3, ch = c & 7;
      if (f < ncols)
        *(u32x4*)(z2t + (size_t)(ct * 192 + f) * S_ + r0 + ch * 8) = *(const u32x4*)(img + f * 144 + ch * 16);
    }
  }
}

template <bool Z2>
DEVI void k3_compute(char* smem, u16* __restrict__ z1, u16* __restrict__ z2t,
                     u64* __restrict__ gran, int rt, int ct, int lane, int wn, int tid) {
  f32x2* sst = (f32x2*)(smem + K3_NST * K3_ST);
  f32x4 acc[2][3], acc2[2][3], cs[3];
#pragma unroll
  for (int i = 0; i < 2; ++i)
#pragma unroll
    for (int j = 0; j < 3; ++j) { acc[i][j] = (f32x4){0.f, 0.f, 0.f, 0.f}; acc2[i][j] = acc[i][j]; }
#pragma unroll
  for (int j = 0; j < 3; ++j) cs[j] = (f32x4){0.f, 0.f, 0.f, 0.f};
  const bf16x8 ones = {0x3F80, 0x3F80, 0x3F80, 0x3F80, 0x3F80, 0x3F80, 0x3F80, 0x3F80};
  const int l15 = lane & 15, g4 = lane >> 4;
  int aoff[2], boff[3];
#pragma unroll
  for (int ks = 0; ks < 2; ++ks) aoff[ks] = l15 * 128 + (((4 * ks + g4) ^ (l15 & 7)) << 4);
  const int fl = ((lane >> 3) & 1) | (((lane >> 4) & 1) << 1);
#pragma unroll
  for (int j = 0; j < 3; ++j) {
    int nt = wn * 3 + j;
    boff[j] = K3_AST + (nt >> 2) * 8192 + (8 * g4 + (l15 >> 2)) * 128 + 8 * (lane & 3) + (((nt & 3) ^ fl) << 5);
  }
  f32x4 sm = {0.f, 0.f, 0.f, 0.f}, sg = sm;
  bf16x8 af[2][4], bf[2][3], sf[2];
  auto rdfr = [&](int ks, int buf) {
    const char* sb = smem + buf * K3_ST;
#pragma unroll
    for (int j = 0; j < 3; ++j) bf[ks][j] = lds_tr2(sb + boff[j] + ks * 4096);
#pragma unroll
    for (int mt = 0; mt < 4; ++mt) af[ks][mt] = lds_b128(sb + mt * 2048 + aoff[ks]);
    sf[ks] = lds_b128(sb + wn * 2048 + aoff[ks]);
  };
  auto mm = [&](int ks) {
#pragma unroll
    for (int j = 0; j < 3; ++j) {
      if constexpr (Z2) cs[j] = mfma(ones, bf[ks][j], cs[j]);
      else cs[j] = mfma(bf[ks][j], ones, cs[j]);
    }
#pragma unroll
    for (int mt = 0; mt < 2; ++mt)
#pragma unroll
      for (int j = 0; j < 3; ++j) {
        if constexpr (Z2) { acc[mt][j] = mfma(af[ks][mt], bf[ks][j], acc[mt][j]); acc2[mt][j] = mfma(af[ks][2 + mt], bf[ks][j], acc2[mt][j]); }
        else { acc[mt][j] = mfma(bf[ks][j], af[ks][mt], acc[mt][j]); acc2[mt][j] = mfma(bf[ks][j], af[ks][2 + mt], acc2[mt][j]); }
      }
    sm = mfma(ones, sf[ks], sm); sg = mfma(sf[ks], sf[ks], sg);
  };
  ring_consumer<8, K3_NST, 11, 17>(rdfr, mm);
  bar();
  {
    const f32x2 st = stat_fin(sm, sg, lane);
    if (lane < 16) sst[wn * 16 + lane] = st;
    f32x4* ho = (f32x4*)(smem + K3E_ACC) + wn * 9 * 64 + lane;
#pragma unroll
    for (int i = 0; i < 2; ++i)
#pragma unroll
      for (int j = 0; j < 3; ++j) ho[(i * 3 + j) * 64] = acc2[i][j];
#pragma unroll
    for (int j = 0; j < 3; ++j) ho[(6 + j) * 64] = cs[j];
  }
  bar();
  k3_epi<Z2>(smem, acc, cs, z1, z2t, gran, rt, ct, lane, wn, 0, tid);
}

template <bool Z2>
DEVI void k3_helper(char* smem, u16* __restrict__ z1, u16* __restrict__ z2t,
                    u64* __restrict__ gran, int rt, int ct, int lane, int wn, int tid) {
  bar();
  bar();
  f32x4 acc[2][3], cs[3];
  const f32x4* hi = (const f32x4*)(smem + K3E_ACC) + wn * 9 * 64 + lane;
#pragma unroll
  for (int i = 0; i < 2; ++i)
#pragma unroll
    for (int j = 0; j < 3; ++j) acc[i][j] = hi[(i * 3 + j) * 64];
#pragma unroll
  for (int j = 0; j < 3; ++j) cs[j] = hi[(6 + j) * 64];
  k3_epi<Z2>(smem, acc, cs, z1, z2t, gran, rt, ct, lane, wn, 2, tid);
}

DEVI void phase3(char* smem, const Params& p, const Ctx& c, const u32* dep, bool pre_done) {
  const u16* __restrict__ x = p.x1;
  const int tid = c.tid, lane = c.lane, wid = c.wid, rt = c.rt, c8 = c.c8;
  const int r0 = rt * 64;
  const u32 lb = c.lb;
  if (wid >= 4) {
    const int lw = wid - 4;
    const u32 avoff = (u32)((lane >> 3) * (D_ * 2) + (((lane & 7) ^ (lane >> 3)) << 4));
    ring_loader<8, K3_NST, 2, 6, 2>(
        pre_done,
        [&](int kt, int buf) {
#pragma unroll
          for (int i = 0; i < 2; ++i)
            glds16(x + (size_t)(r0 + 8 * (lw * 2 + i)) * D_ + kt * 64, avoff, lb + buf * K3_ST + (lw * 2 + i) * 1024);
        },
        [&](int kt, int buf) { p3_ldB(p, c, kt, buf); });
    if (c8 < 4) k3_helper<false>(smem, p.z1, p.z2t, p.gran, rt, c8, lane, lw, tid);
    else k3_helper<true>(smem, p.z1, p.z2t, p.gran, rt, c8 - 4, lane, lw, tid);
  } else {
    if (wid == 0 && dep != nullptr) dep_wait(dep);
    if (c8 < 4) k3_compute<false>(smem, p.z1, p.z2t, p.gran, rt, c8, lane, wid, tid);
    else k3_compute<true>(smem, p.z1, p.z2t, p.gran, rt, c8 - 4, lane, wid, tid);
  }
}

constexpr int K4_ST = (64 + 96) * 256;
constexpr int K4_NST = 4;
constexpr int K4_NT = 16;

DEVI void phase4(char* smem, const Params& p, const Ctx& c, u32* sig, int next, const Ctx& cn) {
  const int lane = c.lane, wid = c.wid, nt0 = c.c8;
  const int r0 = c.rt * 64;
  const int u0 = nt0 * 6 < 37 ? nt0 * 6 : 37;
  const int f0 = u0 * 16;
  if (wid >= 4) {
    const int lw = wid - 4;
    const u16* asrc = p.w_sp + (size_t)(r0 + 16 * lw) * S_;
    const u16* bsrc = p.z2t_in + (size_t)(f0 + 24 * lw) * S_;
    const u32 lb = c.lb;
    u32 voff[4];
#pragma unroll
    for (int i = 0; i < 4; ++i) voff[i] = (u32)((lane >> 4) * (S_ * 2) + (((lane & 15) ^ (4 * i + (lane >> 4))) << 4));
    const int bsh = (lw & 1) * 2;
    ring_loader<K4_NT, K4_NST, 4, 6, K4_NST - 1>(
        false,
        [&](int kt, int buf) {
#pragma unroll
          for (int i = 0; i < 4; ++i) glds16(asrc + (size_t)(4 * i) * S_ + kt * 128, voff[i], lb + buf * K4_ST + (lw * 4 + i) * 1024);
        },
        [&](int kt, int buf) {
#pragma unroll
          for (int i = 0; i < 6; ++i) {
            const u32 v = bsh ? voff[(i + 2) & 3] : voff[i & 3];
            glds16(bsrc + (size_t)(4 * i) * S_ + kt * 128, v, lb + buf * K4_ST + 16384 + (lw * 6 + i) * 1024);
          }
        });
    bar();
    next_pre(next, p, cn);
  } else {
    const int l15 = lane & 15, g4 = lane >> 4;
    const int wm = wid >> 1, wn = wid & 1;
    u32x2 zv[2][3];
    float bs[2];
#pragma unroll
    for (int i = 0; i < 2; ++i) {
      bs[i] = bf2f(p.b_sp[r0 + wm * 32 + i * 16 + l15]);
#pragma unroll
      for (int j = 0; j < 3; ++j)
        zv[i][j] = *(const u32x2*)(p.z1_in + (size_t)(r0 + wm * 32 + i * 16 + l15) * FH + f0 + wn * 48 + j * 16 + 4 * g4);
    }
    f32x4 acc[2][3];
#pragma unroll
    for (int i = 0; i < 2; ++i)
#pragma unroll
      for (int j = 0; j < 3; ++j) acc[i][j] = (f32x4){0.f, 0.f, 0.f, 0.f};
    int xo[4];
#pragma unroll
    for (int k4 = 0; k4 < 4; ++k4) xo[k4] = l15 * 256 + (((4 * k4 + g4) ^ l15) << 4);
    const int abase = wm * 32 * 256, bbase = 16384 + wn * 48 * 256;
    bf16x8 af[2][2][2], bf[2][2][3];
    ring_consumer<K4_NT, K4_NST, 10, 12>(
        [&](int hs, int buf) {
          const char* sb = smem + buf * K4_ST;
#pragma unroll
          for (int ks = 0; ks < 2; ++ks) {
#pragma unroll
            for (int i = 0; i < 2; ++i) af[hs][ks][i] = lds_b128(sb + abase + i * 4096 + xo[2 * hs + ks]);
#pragma unroll
            for (int j = 0; j < 3; ++j) bf[hs][ks][j] = lds_b128(sb + bbase + j * 4096 + xo[2 * hs + ks]);
          }
        },
        [&](int hs) {
#pragma unroll
          for (int ks = 0; ks < 2; ++ks)
#pragma unroll
            for (int i = 0; i < 2; ++i)
#pragma unroll
              for (int j = 0; j < 3; ++j) acc[i][j] = mfma(bf[hs][ks][j], af[hs][ks][i], acc[i][j]);
        });
    bar();
    const int umin = nt0 * 6;
#pragma unroll
    for (int i = 0; i < 2; ++i) {
      u16* hp = p.h + (size_t)(r0 + wm * 32 + i * 16 + l15) * FHP;
#pragma unroll
      for (int j = 0; j < 3; ++j) {
        int u = u0 + wn * 3 + j;
        if (u >= umin) {
          u32x2 z = zv[i][j], ov;
          float g0 = rbf(rbf(acc[i][j][0]) + bs[i]), g1 = rbf(rbf(acc[i][j][1]) + bs[i]);
          float g2 = rbf(rbf(acc[i][j][2]) + bs[i]), g3 = rbf(rbf(acc[i][j][3]) + bs[i]);
          ov[0] = pack2(bflo(z[0]) * g0, bfhi(z[0]) * g1);
          ov[1] = pack2(bflo(z[1]) * g2, bfhi(z[1]) * g3);
          st8(hp + u * 16 + 4 * g4, ov);
        }
      }
      if (nt0 == 7 && wn == 1) st8(hp + FH + 4 * g4, (u32x2){0u, 0u});
    }
  }
  publish(sig, wid < 4);
}

__global__ __launch_bounds__(512, 2) void fused_phases(const Params p) {
    __shared__ __attribute__((aligned(16))) char smem[163840];
  Ctx c;
  c.tid = threadIdx.x; c.lane = c.tid & 63;
  c.wid = __builtin_amdgcn_readfirstlane(c.tid >> 6);
  const int bid = blockIdx.x, xcd = bid & 7, idx = bid >> 3;
  c.rt = (xcd >> 1) * 8 + (idx & 7);
  c.c8 = (xcd & 1) * 4 + (idx >> 3);
  c.lb = lds_addr(smem);
  const int ph = p.phases;
  u32* cnt = p.cnt + c.rt * 32;
#define DS(b)
  GSTAMP(lid, 0);
#define P4MAP 0
  if (ph & 1) {
    DS(6);
    Ctx c4 = c;
    if (P4MAP == 1) { c4.rt = xcd * 4 + (idx & 3); c4.c8 = idx >> 2; }
    if (P4MAP == 2) { c4.rt = (xcd >> 2) * 16 + (idx & 15); c4.c8 = (xcd & 3) * 2 + (idx >> 4); }
    if (P4MAP == 3) { c4.rt = bid >> 3; c4.c8 = bid & 7; }
    phase4(smem, p, c4, p.cnt + c4.rt * 32, 5, c);
    GSTAMP(lid, 1);
  }
  if (ph & 2) { DS(9); phase_proj<FHP, 11, FH>(smem, c, p.h, p.w_out, p.x1_in, p.xout, cnt, (ph & 4) ? cnt + 1024 : nullptr, p, true, (ph & 4) ? 1 : 0); GSTAMP(lid, 2); }
  if (ph & 4) {
    DS(12);
    phase1(smem, p, c, (ph & 2) ? cnt + 1024 : nullptr, ((ph & 2) && c.rt > 0) ? cnt + 1024 - 32 : nullptr,
           (ph & 8) ? cnt + 2048 : nullptr, (ph & 2) != 0, (ph & 8) ? 2 : 0);
    GSTAMP(lid, 3);
  }
  if (ph & 8) { DS(15); phase_proj<D_, 8, D_>(smem, c, p.o, p.wo, p.xin, p.x1, (ph & 4) ? cnt + 2048 : nullptr, cnt + 3072, p, (ph & 4) != 0, 3); GSTAMP(lid, 4); }
  if (ph & 16) { DS(18); phase3(smem, p, c, cnt + 3072, true); GSTAMP(lid, 5); }
}

extern "C" void kernel_launch(void* const* d_in, const int* in_sizes, int n_in, void* d_out, int out_size,
                              void* d_ws, size_t ws_size, hipStream_t stream) {
  char* ws = (char*)d_ws;
  constexpr size_t MB = 1 << 20;
  u64* gran = (u64*)ws;
  u32* cnt = (u32*)(ws + 256 * 1024);
  const int zero_words = (256 * 1024 + 3 * 16384) / 4;
  auto buf = [&](int i) { return (u16*)(ws + (1 + 3 * i) * MB); };
  u16 *o[2] = {buf(0), buf(1)}, *x1[2] = {buf(2), buf(3)}, *z1[2] = {buf(4), buf(5)}, *z2t[2] = {buf(6), buf(7)};
  u16 *h[2] = {buf(8), buf(9)}, *x2 = buf(10);

  Params p;
  p.wqkv = (const u16*)d_in[1]; p.wo = (const u16*)d_in[2]; p.w_in = (const u16*)d_in[4];
  p.w_sp = (const u16*)d_in[5]; p.b_sp = (const u16*)d_in[6]; p.w_out = (const u16*)d_in[7];
  p.zero_area = (u32*)ws;
  p.z2t_in = z2t[0]; p.z1_in = z1[0]; p.x1_in = x1[0]; p.h = h[0]; p.xout = x2;
  p.xin = (const u16*)d_in[0]; p.o = o[0]; p.x1 = x1[0]; p.z1 = z1[0]; p.z2t = z2t[0]; p.gran = gran;
  p.cnt = cnt; p.zero_words = zero_words; p.phases = 4;
  fused_phases<<<256, 512, 0, stream>>>(p);
  p.zero_words = 0; p.phases = 24;
  fused_phases<<<256, 512, 0, stream>>>(p);
  p.xin = x2; p.o = o[1]; p.x1 = x1[1]; p.z1 = z1[1]; p.z2t = z2t[1]; p.gran = gran + 16384;
  p.cnt = cnt + 4096; p.phases = 31;
  fused_phases<<<256, 512, 0, stream>>>(p);
  p.z2t_in = z2t[1]; p.z1_in = z1[1]; p.x1_in = x1[1]; p.h = h[1]; p.xout = (u16*)d_out;
  p.cnt = cnt + 8192; p.phases = 3;
  fused_phases<<<256, 512, 0, stream>>>(p);
}
```

```cpp
#include <hip/hip_runtime.h>
#include <hip/hip_bf16.h>
#include <stdint.h>

typedef __attribute__((ext_vector_type(8))) short bf16x8;
typedef __attribute__((ext_vector_type(4))) short s16x4;
typedef __attribute__((ext_vector_type(4))) float f32x4;
typedef __attribute__((ext_vector_type(2))) float f32x2;
typedef __attribute__((ext_vector_type(2))) __bf16 bf16v2;
typedef __attribute__((ext_vector_type(4))) unsigned u32x4;
typedef __attribute__((ext_vector_type(2))) unsigned u32x2;
typedef unsigned short u16;
typedef unsigned int u32;
typedef unsigned long long u64;

#define LDS_AS __attribute__((address_space(3)))
#define GLB_AS __attribute__((address_space(1)))
#define DEVI __device__ __forceinline__

constexpr int S_ = 2048, D_ = 512, FH = 688, FHP = 704, F2 = 1376;

DEVI float bf2f(u32 v) { return __uint_as_float(v << 16); }
DEVI float bflo(u32 w) { return __uint_as_float(w << 16); }
DEVI float bfhi(u32 w) { return __uint_as_float(w & 0xffff0000u); }
DEVI u32 pack2(float a, float b) {
  bf16v2 v; v[0] = (__bf16)a; v[1] = (__bf16)b;
  return __builtin_bit_cast(u32, v);
}
DEVI float dot2(u32 a, u32 b, float c) {
  return __builtin_amdgcn_fdot2_f32_bf16(__builtin_bit_cast(bf16v2, a), __builtin_bit_cast(bf16v2, b), c, false);
}
DEVI float rbf(float f) { return bf2f((u32)__builtin_bit_cast(u16, (__bf16)f)); }

DEVI void glds16(const void* sbase_, u32 voff, u32 lds_) {
  u32 keep;
  const u64 sb_ = (u64)sbase_;
  const u64 sbase = ((u64)(u32)__builtin_amdgcn_readfirstlane((int)(sb_ >> 32)) << 32) |
                    (u64)(u32)__builtin_amdgcn_readfirstlane((int)sb_);
  const u32 lds = (u32)__builtin_amdgcn_readfirstlane((int)lds_);
  asm volatile("s_mov_b32 %0, m0\n\ts_mov_b32 m0, %3\n\ts_nop 0\n\tglobal_load_lds_dwordx4 %1, %2\n\ts_mov_b32 m0, %0"
               : "=&s"(keep) : "v"(voff), "s"(sbase), "s"(lds) : "memory");
}
DEVI u32 lds_addr(const void* p) { return (u32)(size_t)(LDS_AS const char*)p; }
template <int N> DEVI void wait_vm() { asm volatile("s_waitcnt vmcnt(%0)" ::"n"(N) : "memory"); }
#define GSTAMP(k, i)
#define GS2(i)
DEVI void wait_lgkm0() {
  __builtin_amdgcn_sched_barrier(0);
  __builtin_amdgcn_s_waitcnt(0xC07F);
  asm volatile("" ::: "memory");
  __builtin_amdgcn_sched_barrier(0);
}
DEVI void bar() { wait_lgkm0(); __builtin_amdgcn_s_barrier(); __builtin_amdgcn_sched_barrier(0); }

DEVI void st8(void* p, u32x2 v) {
  __hip_atomic_store((u64*)p, ((u64)v[1] << 32) | (u64)v[0], __ATOMIC_RELAXED, __HIP_MEMORY_SCOPE_AGENT);
}
DEVI void st16(void* p, u32x4 v) {
  asm volatile("global_store_dwordx4 %0, %1, off sc1\n\ts_nop 1" ::"v"(p), "v"(v) : "memory");
}
DEVI void publish(u32* cnt, bool stored) {
  if (stored) wait_vm<0>();
  bar();
  if (cnt != nullptr && threadIdx.x == 0) __hip_atomic_fetch_add(cnt, 1u, __ATOMIC_RELAXED, __HIP_MEMORY_SCOPE_AGENT);
}
DEVI void dep_wait(const u32* cnt) {
  int spins = 0;
  while (__hip_atomic_load(cnt, __ATOMIC_RELAXED, __HIP_MEMORY_SCOPE_AGENT) < 8u && ++spins < (1 << 21))
    __builtin_amdgcn_s_sleep(1);
}

template <int I> struct IC { static constexpr int v = I; };
template <int N, int I = 0, class F> DEVI void sfor(F&& f) {
  if constexpr (I < N) { f(IC<I>{}); sfor<N, I + 1>(f); }
}

template <int NRD, int NMM> DEVI void interleave() {
  constexpr int NP = NRD < NMM ? NRD : NMM;
#pragma unroll
  for (int i = 0; i < NP; ++i) {
    __builtin_amdgcn_sched_group_barrier(0x008, 1, 0);
    __builtin_amdgcn_sched_group_barrier(0x100, 1, 0);
  }
  if constexpr (NMM > NP) __builtin_amdgcn_sched_group_barrier(0x008, NMM - NP, 0);
  if constexpr (NRD > NP) __builtin_amdgcn_sched_group_barrier(0x100, NRD - NP, 0);
}
template <int NT, int NST, int NRD, int NMM, class Rd, class Mm> DEVI void ring_consumer(Rd&& rdfr, Mm&& mm) {
  bar();
  GS2(0);
  bar();
  GS2(1);
  rdfr(0, 0);
  int rd = 0;
#pragma unroll 1
  for (int kt = 0; kt < NT - 1; ++kt) {
    wait_lgkm0();
    rdfr(1, rd);
    mm(0);
    interleave<NRD, NMM>();
    rd = rd + 1 == NST ? 0 : rd + 1;
    bar();
    rdfr(0, rd);
    mm(1);
    interleave<NRD, NMM>();
  }
  wait_lgkm0();
  rdfr(1, rd);
  mm(0);
  interleave<NRD, NMM>();
  wait_lgkm0();
  mm(1);
  GS2(2);
}
template <int NT, int NST, int LA, int LB, int PRE, class LdA, class LdB>
DEVI void ring_loader(bool pre_done, LdA&& ldA, LdB&& ldB) {
  static_assert(PRE + 1 <= NST && PRE + 1 <= NT, "ring");
  constexpr int LPW = LA + LB;
  if (!pre_done) {
#pragma unroll 1
    for (int i = 0; i < PRE; ++i) ldB(i, i);
  }
  bar();
#pragma unroll 1
  for (int i = 0; i < PRE; ++i) ldA(i, i);
  wait_vm<LA * (PRE - 1)>();
  bar();
  ldA(PRE, PRE); ldB(PRE, PRE);
  int wr = PRE + 1 == NST ? 0 : PRE + 1;
  sfor<PRE - 1>([&](auto ic) {
    constexpr int kt = decltype(ic)::v;
    constexpr int nfull = (kt + 1 < NT - PRE ? kt + 1 : NT - PRE);
    wait_vm<LA * (PRE - 2 - kt) + LPW * nfull>();
    bar();
    if constexpr (kt + PRE + 1 < NT) {
      ldA(kt + PRE + 1, wr); ldB(kt + PRE + 1, wr);
      wr = wr + 1 == NST ? 0 : wr + 1;
    }
  });
#pragma unroll 1
  for (int kt = PRE - 1; kt < NT - 1 - PRE; ++kt) {
    wait_vm<LPW * (PRE - 1)>();
    bar();
    ldA(kt + PRE + 1, wr); ldB(kt + PRE + 1, wr);
    wr = wr + 1 == NST ? 0 : wr + 1;
  }
  constexpr int K0 = (PRE - 1 > NT - 1 - PRE) ? PRE - 1 : NT - 1 - PRE;
  sfor<NT - 1 - K0>([&](auto ic) {
    constexpr int kt = K0 + decltype(ic)::v;
    wait_vm<LPW * (NT - 2 - kt)>();
    bar();
  });
}

template <int CTRL> DEVI float dpp_f(float x) {
  return __builtin_bit_cast(float, __builtin_amdgcn_update_dpp(0, __builtin_bit_cast(int, x), CTRL, 0xF, 0xF, true));
}
DEVI float row16_sum(float x) {
  x += dpp_f<0xB1>(x);
  x += dpp_f<0x4E>(x);
  x += dpp_f<0x141>(x);
  x += dpp_f<0x140>(x);
  return x;
}

DEVI bf16x8 lds_b128(const char* p) { return *(const bf16x8*)p; }
DEVI bf16x8 lds_tr2(const char* p) {
  s16x4 a = __builtin_amdgcn_ds_read_tr16_b64_v4i16((LDS_AS s16x4*)p);
  s16x4 b = __builtin_amdgcn_ds_read_tr16_b64_v4i16((LDS_AS s16x4*)(p + 512));
  return __builtin_shufflevector(a, b, 0, 1, 2, 3, 4, 5, 6, 7);
}
DEVI f32x4 mfma(bf16x8 a, bf16x8 b, f32x4 c) { return __builtin_amdgcn_mfma_f32_16x16x32_bf16(a, b, c, 0, 0, 0); }

DEVI f32x2 gelu2(f32x2 x) {
  const f32x2 c1 = {-2.3022081985f, -2.3022081985f};
  const f32x2 c2 = {-0.10294324f, -0.10294324f};
  const f32x2 one = {1.0f, 1.0f};
  f32x2 a = x * (x * x * c2 + c1);
  f32x2 e = {__builtin_amdgcn_exp2f(a[0]), __builtin_amdgcn_exp2f(a[1])};
  f32x2 d = e + one;
  f32x2 r = {__builtin_amdgcn_rcpf(d[0]), __builtin_amdgcn_rcpf(d[1])};
  return x * r;
}
DEVI f32x2 unpk(u32 w) { return (f32x2){bflo(w), bfhi(w)}; }

DEVI f32x2 stat_fin(const f32x4& sm, const f32x4& sg, int lane) {
  const int j = lane & 3;
  float dg = j == 0 ? sg[0] : (j == 1 ? sg[1] : (j == 2 ? sg[2] : sg[3]));
  const float q = __shfl(dg, (lane & 15) + 16 * ((lane & 15) >> 2));
  const float mean = sm[0] * (1.0f / 512.0f);
  const float var = fmaxf(q * (1.0f / 512.0f) - mean * mean, 0.f);
  const float sd = rbf(__builtin_sqrtf(rbf(rbf(var) + 1e-5f)));
  return (f32x2){rbf(mean), 1.0f / sd};
}

struct Params {
  const u16 *wqkv, *wo, *w_in, *w_sp, *b_sp, *w_out;
  const u16 *z2t_in, *z1_in, *x1_in;
  u16 *h, *xout;
  const u16* xin;
  u16 *o, *x1, *z1, *z2t;
  u64* gran;
  u32* cnt;
  u32* zero_area;
  int zero_words, phases;
};
struct Ctx { int tid, lane, wid, rt, c8; u32 lb; };

constexpr int K1_AST = 80 * 128;
constexpr int K1_ST = K1_AST + 64 * 192 * 2;
constexpr int K1_NST = 4;
constexpr int K3_AST = 64 * 128;
constexpr int K3_ST = K3_AST + 64 * 192 * 2;
constexpr int K3_NST = 4;
constexpr int PJ_ST = 16384, PJ_NST = 7, PJ_PRE = 6, PJ_BASE = 40960;

DEVI void p1_ldB(const Params& p, const Ctx& c, int kt, int buf) {
  const int lw = c.wid - 4, lane = c.lane;
#pragma unroll
  for (int i = 0; i < 6; ++i) {
    int pi = lw * 6 + i, panel = pi >> 3, pp = pi & 7;
    int f = ((lane >> 4) & 1) | ((i & 1) << 1);
    int sc = ((((lane & 7) >> 1) ^ f) << 1) | (lane & 1);
    glds16(p.wqkv + (size_t)(kt * 64 + 8 * pp) * 1536 + panel * 512 + c.c8 * 64, (u32)((lane >> 3) * 3072 + sc * 16),
           c.lb + buf * K1_ST + K1_AST + panel * 8192 + pp * 1024);
  }
}
DEVI void p3_ldB(const Params& p, const Ctx& c, int kt, int buf) {
  const int lw = c.wid - 4, lane = c.lane;
  const int cbase = (c.c8 >> 2) * FH + (c.c8 & 3) * 192;
#pragma unroll
  for (int i = 0; i < 6; ++i) {
    int pi = lw * 6 + i, panel = pi >> 3, pp = pi & 7;
    int f = ((lane >> 4) & 1) | ((i & 1) << 1);
    int sc = ((((lane & 7) >> 1) ^ f) << 1) | (lane & 1);
    int col = cbase + panel * 64 + sc * 8;
    col = col <= F2 - 8 ? col : F2 - 8;
    glds16(p.w_in + (size_t)(kt * 64 + 8 * pp) * F2, (u32)((lane >> 3) * (F2 * 2) + col * 2),
           c.lb + buf * K3_ST + K3_AST + panel * 8192 + pp * 1024);
  }
}
DEVI void pj_ldB(const u16* W, int KW, const Ctx& c, int kt, int buf) {
  const int lw = c.wid - 4, lane = c.lane;
#pragma unroll
  for (int i = 0; i < 2; ++i) {
    int f = ((lane >> 4) & 1) | (i << 1);
    int sc = ((((lane & 7) >> 1) ^ f) << 1) | (lane & 1);
    int krow = kt * 64 + 8 * (lw * 2 + i) + (lane >> 3);
    krow = krow < KW ? krow : KW - 1;
    glds16(W + c.c8 * 64, (u32)(krow * (D_ * 2) + sc * 16), c.lb + PJ_BASE + buf * PJ_ST + 8192 + (lw * 2 + i) * 1024);
  }
}
DEVI void next_pre(int next, const Params& p, const Ctx& c) {
  if (next == 5) {
#pragma unroll 1
    for (int i = 0; i < PJ_PRE; ++i) pj_ldB(p.w_out, FH, c, i, i);
  } else if (next == 2) {
#pragma unroll 1
    for (int i = 0; i < PJ_PRE; ++i) pj_ldB(p.wo, D_, c, i, i);
  } else if (next == 1) {
    p1_ldB(p, c, 0, 0); p1_ldB(p, c, 1, 1);
  } else if (next == 3) {
    p3_ldB(p, c, 0, 0); p3_ldB(p, c, 1, 1);
  }
}

DEVI void phase1(char* smem, const Params& p, const Ctx& c, const u32* dep0, const u32* dep1, u32* sig,
                 bool pre_done, int next) {
  f32x2* sst = (f32x2*)(smem + 31744);
  const u16* __restrict__ x = p.xin;
  const int tid = c.tid, lane = c.lane, wid = c.wid, hg = c.c8;
  const int r0 = c.rt * 64;
  const u32 lb = c.lb;
  const int hw = wid & 3, mh = wid >> 2;
  const int l15 = lane & 15, g4 = lane >> 4;
  constexpr int QH = 1152, KH = 1408, HSZ = 2 * QH + 4 * KH;
  if (wid >= 4) {
    const int lw = wid - 4;
    const int ap0 = lw < 2 ? lw * 3 : 6 + (lw - 2) * 2;
    u32 avoff[3];
#pragma unroll
    for (int i = 0; i < 3; ++i) {
      int gr = r0 - 16 + 8 * (ap0 + i) + (lane >> 3); gr = gr < 0 ? 0 : (gr > S_ - 1 ? S_ - 1 : gr);
      avoff[i] = (u32)(gr * (D_ * 2) + (((lane & 7) ^ (lane >> 3)) << 4));
    }
    auto ldB = [&](int kt, int buf) { p1_ldB(p, c, kt, buf); };
    auto ldA3 = [&](int kt, int buf) {
#pragma unroll
      for (int i = 0; i < 3; ++i) glds16(x + kt * 64, avoff[i], lb + buf * K1_ST + (ap0 + i) * 1024);
    };
    auto ldA2 = [&](int kt, int buf) {
#pragma unroll
      for (int i = 0; i < 2; ++i) glds16(x + kt * 64, avoff[i], lb + buf * K1_ST + (ap0 + i) * 1024);
    };
    if (lw < 2) ring_loader<8, K1_NST, 3, 6, 2>(pre_done, ldA3, ldB);
    else ring_loader<8, K1_NST, 2, 6, 2>(pre_done, ldA2, ldB);
    for (int i = blockIdx.x * 256 + (tid - 256); i < p.zero_words; i += 256 * 256) p.zero_area[i] = 0u;
    bar();
    next_pre(next, p, c);
    bar();
  } else {
    if (wid == 0 && dep0 != nullptr) { dep_wait(dep0); if (dep1 != nullptr) dep_wait(dep1); }
    if (p.zero_words != 0) {
      const u32 junk = lb + K1_NST * K1_ST + wid * 1024;
      const u32 vo = (u32)(lane * 16);
      const int b = blockIdx.x, bc = b < 172 ? b : 171;
      glds16((const char*)p.wo + (size_t)b * 2048 + (wid & 1) * 1024, vo, junk);
      glds16((const char*)p.w_in + (size_t)bc * 8192 + wid * 2048, vo, junk);
      glds16((const char*)p.w_in + (size_t)bc * 8192 + wid * 2048 + 1024, vo, junk);
#pragma unroll 1
      for (int i = wid; i < 32; i += 4) glds16((const char*)p.w_sp + (size_t)b * 32768 + i * 1024, vo, junk);
      glds16((const char*)p.w_out + (size_t)bc * 4096 + wid * 1024, vo, junk);
    }
    f32x4 acc[14], cs[3];
#pragma unroll
    for (int i = 0; i < 14; ++i) acc[i] = (f32x4){0.f, 0.f, 0.f, 0.f};
#pragma unroll
    for (int i = 0; i < 3; ++i) cs[i] = (f32x4){0.f, 0.f, 0.f, 0.f};
    const bf16x8 ones = {0x3F80, 0x3F80, 0x3F80, 0x3F80, 0x3F80, 0x3F80, 0x3F80, 0x3F80};
    int aoff[2];
#pragma unroll
    for (int ks = 0; ks < 2; ++ks) aoff[ks] = l15 * 128 + (((4 * ks + g4) ^ (l15 & 7)) << 4);
    const int fl = ((lane >> 3) & 1) | (((lane >> 4) & 1) << 1);
    const int boff = K1_AST + (8 * g4 + (l15 >> 2)) * 128 + 8 * (lane & 3) + ((hw ^ fl) << 5);
    f32x4 sm = {0.f, 0.f, 0.f, 0.f}, sg = sm, sm4 = sm, sg4 = sm;
    bf16x8 af[2][5], bf[2][3], sf[2];
    auto rdfr = [&](int ks, int buf) {
      const char* sb = smem + buf * K1_ST;
#pragma unroll
      for (int pn = 0; pn < 3; ++pn) bf[ks][pn] = lds_tr2(sb + boff + pn * 8192 + ks * 4096);
#pragma unroll
      for (int mt = 0; mt < 5; ++mt) af[ks][mt] = lds_b128(sb + mt * 2048 + aoff[ks]);
      sf[ks] = lds_b128(sb + hw * 2048 + aoff[ks]);
    };
    auto mm = [&](int ks) {
#pragma unroll
      for (int pn = 0; pn < 3; ++pn) cs[pn] = mfma(bf[ks][pn], ones, cs[pn]);
#pragma unroll
      for (int mt = 0; mt < 5; ++mt) {
        if (mt > 0) acc[mt - 1] = mfma(bf[ks][0], af[ks][mt], acc[mt - 1]);
        acc[4 + mt] = mfma(bf[ks][1], af[ks][mt], acc[4 + mt]);
        acc[9 + mt] = mfma(bf[ks][2], af[ks][mt], acc[9 + mt]);
      }
      sm = mfma(ones, sf[ks], sm); sg = mfma(sf[ks], sf[ks], sg);
      sm4 = mfma(ones, af[ks][4], sm4); sg4 = mfma(af[ks][4], af[ks][4], sg4);
    };
    ring_consumer<8, K1_NST, 12, 21>(rdfr, mm);
    bar();
    {
      const f32x2 st = stat_fin(sm, sg, lane), st4 = stat_fin(sm4, sg4, lane);
      if (lane < 16) { sst[hw * 16 + lane] = st; if (hw == 0) sst[64 + lane] = st4; }
    }
    bar();
    char* sw = smem + hw * HSZ;
    char* sq = sw; char* sk = sw + 2 * QH; char* sv = sk + 2 * KH;
    const int half = g4 >> 1, o8 = (g4 & 1) * 8;
#pragma unroll
    for (int mt = 0; mt < 5; ++mt) {
      const f32x2 ms = sst[mt * 16 + l15];
      auto put = [&](char* base, int hs, int row, const f32x4& a, const f32x4& c) {
        u32x2 pq = {pack2((a[0] - ms[0] * c[0]) * ms[1], (a[1] - ms[0] * c[1]) * ms[1]),
                    pack2((a[2] - ms[0] * c[2]) * ms[1], (a[3] - ms[0] * c[3]) * ms[1])};
        *(u32x2*)(base + half * hs + row * 16 + o8) = pq;
      };
      if (mt > 0) put(sq, QH, (mt - 1) * 16 + l15, acc[mt - 1], cs[0]);
      put(sk, KH, mt * 16 + l15, acc[4 + mt], cs[1]);
      put(sv, KH, mt * 16 + l15, acc[9 + mt], cs[2]);
    }
  }
  bar();
  {
    const int hf = lane & 1, tl = mh * 32 + (lane >> 1);
    const int t = r0 + tl;
    const char* sq = smem + hw * HSZ + hf * QH;
    const char* sk = smem + hw * HSZ + 2 * QH + hf * KH;
    const char* sv = sk + 2 * KH;
    u32x4 q0 = *(const u32x4*)(sq + tl * 16);
    float lg[8];
    float mx = -3.0e38f;
#pragma unroll
    for (int j = 0; j < 8; ++j) {
      u32x4 k0 = *(const u32x4*)(sk + (tl + 16 - 2 * j) * 16);
      float d = 0.f;
#pragma unroll
      for (int e = 0; e < 4; ++e) d = dot2(q0[e], k0[e], d);
      d += dpp_f<0xB1>(d);
      d = rbf(d) * 0.25f;
      d = (t - 2 * j >= 0) ? d : -1.0e9f;
      lg[j] = d;
      mx = fmaxf(mx, d);
    }
    float sum = 0.f;
#pragma unroll
    for (int j = 0; j < 8; ++j) { lg[j] = __expf(lg[j] - mx); sum += lg[j]; }
    const float rs = 1.0f / sum;
    float ov[8];
#pragma unroll
    for (int e = 0; e < 8; ++e) ov[e] = 0.f;
#pragma unroll
    for (int jp = 0; jp < 4; ++jp) {
      const u32 pp = pack2(lg[2 * jp] * rs, lg[2 * jp + 1] * rs);
      int ea = tl + 16 - 4 * jp;
      u32x4 va = *(const u32x4*)(sv + ea * 16), vb = *(const u32x4*)(sv + (ea - 2) * 16);
#pragma unroll
      for (int e = 0; e < 4; ++e) {
        u32 lo = __builtin_amdgcn_perm(vb[e], va[e], 0x05040100u);
        u32 hi = __builtin_amdgcn_perm(vb[e], va[e], 0x07060302u);
        ov[2 * e] = dot2(lo, pp, ov[2 * e]);
        ov[2 * e + 1] = dot2(hi, pp, ov[2 * e + 1]);
      }
    }
    u32x4 w0;
#pragma unroll
    for (int e = 0; e < 4; ++e) w0[e] = pack2(ov[2 * e], ov[2 * e + 1]);
    st16(p.o + (size_t)t * D_ + hg * 64 + hw * 16 + hf * 8, w0);
  }
  publish(sig, true);
}

template <int LDA, int NT, int KW>
DEVI void phase_proj(char* smem, const Ctx& c, const u16* __restrict__ A, const u16* __restrict__ W,
                     const u16* __restrict__ res, u16* __restrict__ out, const u32* dep, u32* sig,
                     const Params& p, bool pre_done, int next) {
  constexpr int ST = PJ_ST, NST = PJ_NST;
  const int lane = c.lane, wid = c.wid;
  const int r0 = c.rt * 64, c0 = c.c8 * 64;
  const u32 lb = c.lb;
  if (wid >= 4) {
    const int lw = wid - 4;
    const u32 avoff = (u32)((lane >> 3) * (LDA * 2) + (((lane & 7) ^ (lane >> 3)) << 4));
    ring_loader<NT, NST, 2, 2, PJ_PRE>(
        pre_done,
        [&](int kt, int buf) {
#pragma unroll
          for (int i = 0; i < 2; ++i)
            glds16(A + (size_t)(r0 + 8 * (lw * 2 + i)) * LDA + kt * 64, avoff, lb + PJ_BASE + buf * ST + (lw * 2 + i) * 1024);
        },
        [&](int kt, int buf) { pj_ldB(W, KW, c, kt, buf); });
    bar();
    next_pre(next, p, c);
  } else {
    const int l15 = lane & 15, g4 = lane >> 4;
    const int wm = wid >> 1, wn = wid & 1;
    u32x2 rv[2][2];
#pragma unroll
    for (int i = 0; i < 2; ++i)
#pragma unroll
      for (int j = 0; j < 2; ++j)
        rv[i][j] = *(const u32x2*)(res + (size_t)(r0 + wm * 32 + i * 16 + l15) * D_ + c0 + wn * 32 + j * 16 + 4 * g4);
    if (wid == 0 && dep != nullptr) dep_wait(dep);
    f32x4 acc[2][2];
#pragma unroll
    for (int i = 0; i < 2; ++i)
#pragma unroll
      for (int j = 0; j < 2; ++j) acc[i][j] = (f32x4){0.f, 0.f, 0.f, 0.f};
    int aoff[2], boff[2];
    const int fl = ((lane >> 3) & 1) | (((lane >> 4) & 1) << 1);
#pragma unroll
    for (int ks = 0; ks < 2; ++ks) aoff[ks] = (wm * 32 + l15) * 128 + (((4 * ks + g4) ^ (l15 & 7)) << 4);
#pragma unroll
    for (int j = 0; j < 2; ++j) boff[j] = 8192 + (8 * g4 + (l15 >> 2)) * 128 + 8 * (lane & 3) + (((wn * 2 + j) ^ fl) << 5);
    bf16x8 af[2][2], bf[2][2];
    ring_consumer<NT, NST, 6, 4>(
        [&](int ks, int buf) {
          const char* sb = smem + PJ_BASE + buf * ST;
#pragma unroll
          for (int i = 0; i < 2; ++i) af[ks][i] = lds_b128(sb + i * 2048 + aoff[ks]);
#pragma unroll
          for (int j = 0; j < 2; ++j) bf[ks][j] = lds_tr2(sb + boff[j] + ks * 4096);
        },
        [&](int ks) {
#pragma unroll
          for (int i = 0; i < 2; ++i)
#pragma unroll
            for (int j = 0; j < 2; ++j) acc[i][j] = mfma(bf[ks][j], af[ks][i], acc[i][j]);
        });
    bar();
#pragma unroll
    for (int i = 0; i < 2; ++i)
#pragma unroll
      for (int j = 0; j < 2; ++j) {
        u32x2 r = rv[i][j], ov;
        ov[0] = pack2(bflo(r[0]) + rbf(acc[i][j][0]), bfhi(r[0]) + rbf(acc[i][j][1]));
        ov[1] = pack2(bflo(r[1]) + rbf(acc[i][j][2]), bfhi(r[1]) + rbf(acc[i][j][3]));
        st8(out + (size_t)(r0 + wm * 32 + i * 16 + l15) * D_ + c0 + wn * 32 + j * 16 + 4 * g4, ov);
      }
  }
  publish(sig, wid < 4);
}

constexpr int K3E_ACC = 0;
constexpr int K3E_ST = 36864;
constexpr int K3E_FIN = 38912;
constexpr int K3E_OUT = 40960;

template <bool Z2>
DEVI void k3_epi(char* smem, f32x4 (&acc)[2][3], const f32x4 (&cs)[3], u16* __restrict__ z1, u16* __restrict__ z2t,
                 u64* __restrict__ gran, int rt, int ct, int lane, int wn, int mt0, int tid) {
  const int r0 = rt * 64;
  const int l15 = lane & 15, g4 = lane >> 4;
  const f32x2* sst = (const f32x2*)(smem + K3_NST * K3_ST);
  const int u0 = ct * 12 + wn * 3;
  int nv = 43 - u0; nv = nv < 0 ? 0 : (nv > 3 ? 3 : nv);
  const int ncols = (ct < 3 ? 12 : 7) * 16;
  u32 zp[2][3][2];
#pragma unroll
  for (int i = 0; i < 2; ++i) {
    f32x2 mn[2], rs[2];
    if constexpr (Z2) {
      const f32x4 a = *(const f32x4*)(sst + (mt0 + i) * 16 + 4 * g4), b = *(const f32x4*)(sst + (mt0 + i) * 16 + 4 * g4 + 2);
      mn[0] = (f32x2){a[0], a[2]}; rs[0] = (f32x2){a[1], a[3]};
      mn[1] = (f32x2){b[0], b[2]}; rs[1] = (f32x2){b[1], b[3]};
    } else {
      const f32x2 ms = sst[(mt0 + i) * 16 + l15];
      mn[0] = mn[1] = (f32x2){ms[0], ms[0]}; rs[0] = rs[1] = (f32x2){ms[1], ms[1]};
    }
#pragma unroll
    for (int j = 0; j < 3; ++j)
#pragma unroll
      for (int hh = 0; hh < 2; ++hh) {
        f32x2 a = {acc[i][j][2 * hh], acc[i][j][2 * hh + 1]}, c = {cs[j][2 * hh], cs[j][2 * hh + 1]};
        f32x2 y = (a - mn[hh] * c) * rs[hh];
        f32x2 g = gelu2(unpk(pack2(y[0], y[1])));
        zp[i][j][hh] = pack2(g[0], g[1]);
      }
  }
  if constexpr (!Z2) {
    char* img = smem + K3E_OUT;
#pragma unroll
    for (int i = 0; i < 2; ++i)
#pragma unroll
      for (int j = 0; j < 3; ++j)
        *(u32x2*)(img + ((mt0 + i) * 16 + l15) * 400 + (wn * 48 + j * 16 + 4 * g4) * 2) = (u32x2){zp[i][j][0], zp[i][j][1]};
    bar();
#pragma unroll
    for (int it = 0; it < 3; ++it) {
      int c = tid + it * 512, row = c / 24, ch = c - row * 24;
      if (ch * 8 < ncols)
        *(u32x4*)(z1 + (size_t)(r0 + row) * FH + ct * 192 + ch * 8) = *(const u32x4*)(img + row * 400 + ch * 16);
    }
  } else {
    f32x2* st = (f32x2*)(smem + K3E_ST);
    f32x2* fin = (f32x2*)(smem + K3E_FIN);
    float sv[16];
#pragma unroll
    for (int i = 0; i < 2; ++i)
#pragma unroll
      for (int hh = 0; hh < 2; ++hh) {
        f32x2 s = {0.f, 0.f}, q = {0.f, 0.f};
#pragma unroll
        for (int j = 0; j < 3; ++j) {
          const f32x2 z = unpk(j < nv ? zp[i][j][hh] : 0u);
          s += z; q += z * z;
        }
        sv[(i * 2 + hh) * 4 + 0] = s[0]; sv[(i * 2 + hh) * 4 + 1] = q[0];
        sv[(i * 2 + hh) * 4 + 2] = s[1]; sv[(i * 2 + hh) * 4 + 3] = q[1];
      }
#pragma unroll
    for (int k = 0; k < 16; ++k) sv[k] += dpp_f<0xB1>(sv[k]);
#pragma unroll
    for (int k = 0; k < 16; ++k) sv[k] += dpp_f<0x4E>(sv[k]);
#pragma unroll
    for (int k = 0; k < 16; ++k) sv[k] += dpp_f<0x141>(sv[k]);
#pragma unroll
    for (int k = 0; k < 16; ++k) sv[k] += dpp_f<0x140>(sv[k]);
    if (l15 == 0) {
#pragma unroll
      for (int i = 0; i < 2; ++i)
#pragma unroll
        for (int hh = 0; hh < 2; ++hh)
          *(f32x4*)(st + wn * 64 + (mt0 + i) * 16 + 4 * g4 + 2 * hh) =
              (f32x4){sv[(i * 2 + hh) * 4], sv[(i * 2 + hh) * 4 + 1], sv[(i * 2 + hh) * 4 + 2], sv[(i * 2 + hh) * 4 + 3]};
    }
    bar();
    if (tid < 64) {
      const f32x2 p = st[tid] + st[64 + tid] + st[128 + tid] + st[192 + tid];
      u64* gbase = gran + (size_t)(rt * 4) * 128;
      __hip_atomic_store(gbase + ct * 128 + tid * 2, (1ull << 32) | (u64)__float_as_uint(p[0]), __ATOMIC_RELAXED, __HIP_MEMORY_SCOPE_AGENT);
      __hip_atomic_store(gbase + ct * 128 + tid * 2 + 1, (1ull << 32) | (u64)__float_as_uint(p[1]), __ATOMIC_RELAXED, __HIP_MEMORY_SCOPE_AGENT);
      float ts, tq;
      int spins = 0;
      bool ok;
      do {
        ok = true; ts = 0.f; tq = 0.f;
#pragma unroll
        for (int c = 0; c < 4; ++c) {
          u64 a = __hip_atomic_load(gbase + c * 128 + tid * 2, __ATOMIC_RELAXED, __HIP_MEMORY_SCOPE_AGENT);
          u64 b = __hip_atomic_load(gbase + c * 128 + tid * 2 + 1, __ATOMIC_RELAXED, __HIP_MEMORY_SCOPE_AGENT);
          ok = ok && (a >> 32) == 1ull && (b >> 32) == 1ull;
          ts += __uint_as_float((u32)a); tq += __uint_as_float((u32)b);
        }
      } while (!ok && ++spins < (1 << 20));
      const float tmean = ts * (1.0f / 688.0f);
      const float vb = rbf(fmaxf(tq * (1.0f / 688.0f) - tmean * tmean, 0.f));
      const float sd = rbf(__builtin_sqrtf(rbf(vb + 1e-5f)));
      fin[tid] = (f32x2){rbf(tmean), 1.0f / sd};
    }
    bar();
    char* img = smem + K3E_OUT;
#pragma unroll
    for (int i = 0; i < 2; ++i) {
      const int row = (mt0 + i) * 16 + 4 * g4;
      const f32x4 a = *(const f32x4*)(fin + row), b = *(const f32x4*)(fin + row + 2);
      const f32x2 mn0 = {a[0], a[2]}, rs0 = {a[1], a[3]}, mn1 = {b[0], b[2]}, rs1 = {b[1], b[3]};
#pragma unroll
      for (int j = 0; j < 3; ++j) {
        f32x2 d0 = unpk(zp[i][j][0]) - mn0, d1 = unpk(zp[i][j][1]) - mn1;
        d0 = unpk(pack2(d0[0], d0[1])) * rs0; d1 = unpk(pack2(d1[0], d1[1])) * rs1;
        *(u32x2*)(img + (wn * 48 + j * 16 + l15) * 144 + row * 2) = (u32x2){pack2(d0[0], d0[1]), pack2(d1[0], d1[1])};
      }
    }
    bar();
#pragma unroll
    for (int it = 0; it < 3; ++it) {
      int c = tid + it * 512, f = c >> 3, ch = c & 7;
      if (f < ncols)
        *(u32x4*)(z2t + (size_t)(ct * 192 + f) * S_ + r0 + ch * 8) = *(const u32x4*)(img + f * 144 + ch * 16);
    }
  }
}

template <bool Z2>
DEVI void k3_compute(char* smem, u16* __restrict__ z1, u16* __restrict__ z2t,
                     u64* __restrict__ gran, int rt, int ct, int lane, int wn, int tid) {
  f32x2* sst = (f32x2*)(smem + K3_NST * K3_ST);
  f32x4 acc[2][3], acc2[2][3], cs[3];
#pragma unroll
  for (int i = 0; i < 2; ++i)
#pragma unroll
    for (int j = 0; j < 3; ++j) { acc[i][j] = (f32x4){0.f, 0.f, 0.f, 0.f}; acc2[i][j] = acc[i][j]; }
#pragma unroll
  for (int j = 0; j < 3; ++j) cs[j] = (f32x4){0.f, 0.f, 0.f, 0.f};
  const bf16x8 ones = {0x3F80, 0x3F80, 0x3F80, 0x3F80, 0x3F80, 0x3F80, 0x3F80, 0x3F80};
  const int l15 = lane & 15, g4 = lane >> 4;
  int aoff[2], boff[3];
#pragma unroll
  for (int ks = 0; ks < 2; ++ks) aoff[ks] = l15 * 128 + (((4 * ks + g4) ^ (l15 & 7)) << 4);
  const int fl = ((lane >> 3) & 1) | (((lane >> 4) & 1) << 1);
#pragma unroll
  for (int j = 0; j < 3; ++j) {
    int nt = wn * 3 + j;
    boff[j] = K3_AST + (nt >> 2) * 8192 + (8 * g4 + (l15 >> 2)) * 128 + 8 * (lane & 3) + (((nt & 3) ^ fl) << 5);
  }
  f32x4 sm = {0.f, 0.f, 0.f, 0.f}, sg = sm;
  bf16x8 af[2][4], bf[2][3], sf[2];
  auto rdfr = [&](int ks, int buf) {
    const char* sb = smem + buf * K3_ST;
#pragma unroll
    for (int j = 0; j < 3; ++j) bf[ks][j] = lds_tr2(sb + boff[j] + ks * 4096);
#pragma unroll
    for (int mt = 0; mt < 4; ++mt) af[ks][mt] = lds_b128(sb + mt * 2048 + aoff[ks]);
    sf[ks] = lds_b128(sb + wn * 2048 + aoff[ks]);
  };
  auto mm = [&](int ks) {
#pragma unroll
    for (int j = 0; j < 3; ++j) {
      if constexpr (Z2) cs[j] = mfma(ones, bf[ks][j], cs[j]);
      else cs[j] = mfma(bf[ks][j], ones, cs[j]);
    }
#pragma unroll
    for (int mt = 0; mt < 2; ++mt)
#pragma unroll
      for (int j = 0; j < 3; ++j) {
        if constexpr (Z2) { acc[mt][j] = mfma(af[ks][mt], bf[ks][j], acc[mt][j]); acc2[mt][j] = mfma(af[ks][2 + mt], bf[ks][j], acc2[mt][j]); }
        else { acc[mt][j] = mfma(bf[ks][j], af[ks][mt], acc[mt][j]); acc2[mt][j] = mfma(bf[ks][j], af[ks][2 + mt], acc2[mt][j]); }
      }
    sm = mfma(ones, sf[ks], sm); sg = mfma(sf[ks], sf[ks], sg);
  };
  ring_consumer<8, K3_NST, 11, 17>(rdfr, mm);
  bar();
  {
    const f32x2 st = stat_fin(sm, sg, lane);
    if (lane < 16) sst[wn * 16 + lane] = st;
    f32x4* ho = (f32x4*)(smem + K3E_ACC) + wn * 9 * 64 + lane;
#pragma unroll
    for (int i = 0; i < 2; ++i)
#pragma unroll
      for (int j = 0; j < 3; ++j) ho[(i * 3 + j) * 64] = acc2[i][j];
#pragma unroll
    for (int j = 0; j < 3; ++j) ho[(6 + j) * 64] = cs[j];
  }
  bar();
  k3_epi<Z2>(smem, acc, cs, z1, z2t, gran, rt, ct, lane, wn, 0, tid);
}

template <bool Z2>
DEVI void k3_helper(char* smem, u16* __restrict__ z1, u16* __restrict__ z2t,
                    u64* __restrict__ gran, int rt, int ct, int lane, int wn, int tid) {
  bar();
  bar();
  f32x4 acc[2][3], cs[3];
  const f32x4* hi = (const f32x4*)(smem + K3E_ACC) + wn * 9 * 64 + lane;
#pragma unroll
  for (int i = 0; i < 2; ++i)
#pragma unroll
    for (int j = 0; j < 3; ++j) acc[i][j] = hi[(i * 3 + j) * 64];
#pragma unroll
  for (int j = 0; j < 3; ++j) cs[j] = hi[(6 + j) * 64];
  k3_epi<Z2>(smem, acc, cs, z1, z2t, gran, rt, ct, lane, wn, 2, tid);
}

DEVI void phase3(char* smem, const Params& p, const Ctx& c, const u32* dep, bool pre_done) {
  const u16* __restrict__ x = p.x1;
  const int tid = c.tid, lane = c.lane, wid = c.wid, rt = c.rt, c8 = c.c8;
  const int r0 = rt * 64;
  const u32 lb = c.lb;
  if (wid >= 4) {
    const int lw = wid - 4;
    const u32 avoff = (u32)((lane >> 3) * (D_ * 2) + (((lane & 7) ^ (lane >> 3)) << 4));
    ring_loader<8, K3_NST, 2, 6, 2>(
        pre_done,
        [&](int kt, int buf) {
#pragma unroll
          for (int i = 0; i < 2; ++i)
            glds16(x + (size_t)(r0 + 8 * (lw * 2 + i)) * D_ + kt * 64, avoff, lb + buf * K3_ST + (lw * 2 + i) * 1024);
        },
        [&](int kt, int buf) { p3_ldB(p, c, kt, buf); });
    if (c8 < 4) k3_helper<false>(smem, p.z1, p.z2t, p.gran, rt, c8, lane, lw, tid);
    else k3_helper<true>(smem, p.z1, p.z2t, p.gran, rt, c8 - 4, lane, lw, tid);
  } else {
    if (wid == 0 && dep != nullptr) dep_wait(dep);
    if (c8 < 4) k3_compute<false>(smem, p.z1, p.z2t, p.gran, rt, c8, lane, wid, tid);
    else k3_compute<true>(smem, p.z1, p.z2t, p.gran, rt, c8 - 4, lane, wid, tid);
  }
}

constexpr int K4_ST = (64 + 96) * 256;
constexpr int K4_NST = 4;
constexpr int K4_NT = 16;

DEVI void phase4(char* smem, const Params& p, const Ctx& c, u32* sig, int next, const Ctx& cn) {
  const int lane = c.lane, wid = c.wid, nt0 = c.c8;
  const int r0 = c.rt * 64;
  const int u0 = nt0 * 6 < 37 ? nt0 * 6 : 37;
  const int f0 = u0 * 16;
  if (wid >= 4) {
    const int lw = wid - 4;
    const u16* asrc = p.w_sp + (size_t)(r0 + 16 * lw) * S_;
    const u16* bsrc = p.z2t_in + (size_t)(f0 + 24 * lw) * S_;
    const u32 lb = c.lb;
    u32 voff[4];
#pragma unroll
    for (int i = 0; i < 4; ++i) voff[i] = (u32)((lane >> 4) * (S_ * 2) + (((lane & 15) ^ (4 * i + (lane >> 4))) << 4));
    const int bsh = (lw & 1) * 2;
    ring_loader<K4_NT, K4_NST, 4, 6, K4_NST - 1>(
        false,
        [&](int kt, int buf) {
#pragma unroll
          for (int i = 0; i < 4; ++i) glds16(asrc + (size_t)(4 * i) * S_ + kt * 128, voff[i], lb + buf * K4_ST + (lw * 4 + i) * 1024);
        },
        [&](int kt, int buf) {
#pragma unroll
          for (int i = 0; i < 6; ++i) {
            const u32 v = bsh ? voff[(i + 2) & 3] : voff[i & 3];
            glds16(bsrc + (size_t)(4 * i) * S_ + kt * 128, v, lb + buf * K4_ST + 16384 + (lw * 6 + i) * 1024);
          }
        });
    bar();
    next_pre(next, p, cn);
  } else {
    const int l15 = lane & 15, g4 = lane >> 4;
    const int wm = wid >> 1, wn = wid & 1;
    u32x2 zv[2][3];
    float bs[2];
#pragma unroll
    for (int i = 0; i < 2; ++i) {
      bs[i] = bf2f(p.b_sp[r0 + wm * 32 + i * 16 + l15]);
#pragma unroll
      for (int j = 0; j < 3; ++j)
        zv[i][j] = *(const u32x2*)(p.z1_in + (size_t)(r0 + wm * 32 + i * 16 + l15) * FH + f0 + wn * 48 + j * 16 + 4 * g4);
    }
    f32x4 acc[2][3];
#pragma unroll
    for (int i = 0; i < 2; ++i)
#pragma unroll
      for (int j = 0; j < 3; ++j) acc[i][j] = (f32x4){0.f, 0.f, 0.f, 0.f};
    int xo[4];
#pragma unroll
    for (int k4 = 0; k4 < 4; ++k4) xo[k4] = l15 * 256 + (((4 * k4 + g4) ^ l15) << 4);
    const int abase = wm * 32 * 256, bbase = 16384 + wn * 48 * 256;
    bf16x8 af[2][2][2], bf[2][2][3];
    ring_consumer<K4_NT, K4_NST, 10, 12>(
        [&](int hs, int buf) {
          const char* sb = smem + buf * K4_ST;
#pragma unroll
          for (int ks = 0; ks < 2; ++ks) {
#pragma unroll
            for (int i = 0; i < 2; ++i) af[hs][ks][i] = lds_b128(sb + abase + i * 4096 + xo[2 * hs + ks]);
#pragma unroll
            for (int j = 0; j < 3; ++j) bf[hs][ks][j] = lds_b128(sb + bbase + j * 4096 + xo[2 * hs + ks]);
          }
        },
        [&](int hs) {
#pragma unroll
          for (int ks = 0; ks < 2; ++ks)
#pragma unroll
            for (int i = 0; i < 2; ++i)
#pragma unroll
              for (int j = 0; j < 3; ++j) acc[i][j] = mfma(bf[hs][ks][j], af[hs][ks][i], acc[i][j]);
        });
    bar();
    const int umin = nt0 * 6;
#pragma unroll
    for (int i = 0; i < 2; ++i) {
      u16* hp = p.h + (size_t)(r0 + wm * 32 + i * 16 + l15) * FHP;
#pragma unroll
      for (int j = 0; j < 3; ++j) {
        int u = u0 + wn * 3 + j;
        if (u >= umin) {
          u32x2 z = zv[i][j], ov;
          float g0 = rbf(rbf(acc[i][j][0]) + bs[i]), g1 = rbf(rbf(acc[i][j][1]) + bs[i]);
          float g2 = rbf(rbf(acc[i][j][2]) + bs[i]), g3 = rbf(rbf(acc[i][j][3]) + bs[i]);
          ov[0] = pack2(bflo(z[0]) * g0, bfhi(z[0]) * g1);
          ov[1] = pack2(bflo(z[1]) * g2, bfhi(z[1]) * g3);
          st8(hp + u * 16 + 4 * g4, ov);
        }
      }
      if (nt0 == 7 && wn == 1) st8(hp + FH + 4 * g4, (u32x2){0u, 0u});
    }
  }
  publish(sig, wid < 4);
}

__global__ __launch_bounds__(512, 2) void fused_phases(const Params p) {
    __shared__ __attribute__((aligned(16))) char smem[163840];
  Ctx c;
  c.tid = threadIdx.x; c.lane = c.tid & 63;
  c.wid = __builtin_amdgcn_readfirstlane(c.tid >> 6);
  const int bid = blockIdx.x, xcd = bid & 7, idx = bid >> 3;
  c.rt = (xcd >> 1) * 8 + (idx & 7);
  c.c8 = (xcd & 1) * 4 + (idx >> 3);
  c.lb = lds_addr(smem);
  const int ph = p.phases;
  u32* cnt = p.cnt + c.rt * 32;
#define DS(b)
  GSTAMP(lid, 0);
#define P4MAP 0
  if (ph & 1) {
    DS(6);
    Ctx c4 = c;
    if (P4MAP == 1) { c4.rt = xcd * 4 + (idx & 3); c4.c8 = idx >> 2; }
    if (P4MAP == 2) { c4.rt = (xcd >> 2) * 16 + (idx & 15); c4.c8 = (xcd & 3) * 2 + (idx >> 4); }
    if (P4MAP == 3) { c4.rt = bid >> 3; c4.c8 = bid & 7; }
    phase4(smem, p, c4, p.cnt + c4.rt * 32, 5, c);
    GSTAMP(lid, 1);
  }
  if (ph & 2) { DS(9); phase_proj<FHP, 11, FH>(smem, c, p.h, p.w_out, p.x1_in, p.xout, cnt, (ph & 4) ? cnt + 1024 : nullptr, p, true, (ph & 4) ? 1 : 0); GSTAMP(lid, 2); }
  if (ph & 4) {
    DS(12);
    phase1(smem, p, c, (ph & 2) ? cnt + 1024 : nullptr, ((ph & 2) && c.rt > 0) ? cnt + 1024 - 32 : nullptr,
           (ph & 8) ? cnt + 2048 : nullptr, (ph & 2) != 0, (ph & 8) ? 2 : 0);
    GSTAMP(lid, 3);
  }
  if (ph & 8) { DS(15); phase_proj<D_, 8, D_>(smem, c, p.o, p.wo, p.xin, p.x1, (ph & 4) ? cnt + 2048 : nullptr, cnt + 3072, p, (ph & 4) != 0, 3); GSTAMP(lid, 4); }
  if (ph & 16) { DS(18); phase3(smem, p, c, cnt + 3072, true); GSTAMP(lid, 5); }
}

extern "C" void kernel_launch(void* const* d_in, const int* in_sizes, int n_in, void* d_out, int out_size,
                              void* d_ws, size_t ws_size, hipStream_t stream) {
  char* ws = (char*)d_ws;
  constexpr size_t MB = 1 << 20;
  u64* gran = (u64*)ws;
  u32* cnt = (u32*)(ws + 256 * 1024);
  const int zero_words = (256 * 1024 + 3 * 16384) / 4;
  auto buf = [&](int i) { return (u16*)(ws + (1 + 3 * i) * MB); };
  u16 *o[2] = {buf(0), buf(1)}, *x1[2] = {buf(2), buf(3)}, *z1[2] = {buf(4), buf(5)}, *z2t[2] = {buf(6), buf(7)};
  u16 *h[2] = {buf(8), buf(9)}, *x2 = buf(10);

  Params p;
  p.wqkv = (const u16*)d_in[1]; p.wo = (const u16*)d_in[2]; p.w_in = (const u16*)d_in[4];
  p.w_sp = (const u16*)d_in[5]; p.b_sp = (const u16*)d_in[6]; p.w_out = (const u16*)d_in[7];
  p.zero_area = (u32*)ws;
  p.z2t_in = z2t[0]; p.z1_in = z1[0]; p.x1_in = x1[0]; p.h = h[0]; p.xout = x2;
  p.xin = (const u16*)d_in[0]; p.o = o[0]; p.x1 = x1[0]; p.z1 = z1[0]; p.z2t = z2t[0]; p.gran = gran;
  p.cnt = cnt; p.zero_words = zero_words; p.phases = 4;
  fused_phases<<<256, 512, 0, stream>>>(p);
  p.zero_words = 0; p.phases = 24;
  fused_phases<<<256, 512, 0, stream>>>(p);
  p.xin = x2; p.o = o[1]; p.x1 = x1[1]; p.z1 = z1[1]; p.z2t = z2t[1]; p.gran = gran + 16384;
  p.cnt = cnt + 4096; p.phases = 31;
  fused_phases<<<256, 512, 0, stream>>>(p);
  p.z2t_in = z2t[1]; p.z1_in = z1[1]; p.x1_in = x1[1]; p.h = h[1]; p.xout = (u16*)d_out;
  p.cnt = cnt + 8192; p.phases = 3;
  fused_phases<<<256, 512, 0, stream>>>(p);
}
```

```cpp
#include <hip/hip_runtime.h>
#include <hip/hip_bf16.h>
#include <stdint.h>

typedef __attribute__((ext_vector_type(8))) short bf16x8;
typedef __attribute__((ext_vector_type(4))) short s16x4;
typedef __attribute__((ext_vector_type(4))) float f32x4;
typedef __attribute__((ext_vector_type(2))) float f32x2;
typedef __attribute__((ext_vector_type(2))) __bf16 bf16v2;
typedef __attribute__((ext_vector_type(4))) unsigned u32x4;
typedef __attribute__((ext_vector_type(2))) unsigned u32x2;
typedef unsigned short u16;
typedef unsigned int u32;
typedef unsigned long long u64;

#define LDS_AS __attribute__((address_space(3)))
#define GLB_AS __attribute__((address_space(1)))
#define DEVI __device__ __forceinline__

constexpr int S_ = 2048, D_ = 512, FH = 688, FHP = 704, F2 = 1376;

DEVI float bf2f(u32 v) { return __uint_as_float(v << 16); }
DEVI float bflo(u32 w) { return __uint_as_float(w << 16); }
DEVI float bfhi(u32 w) { return __uint_as_float(w & 0xffff0000u); }
DEVI u32 pack2(float a, float b) {
  bf16v2 v; v[0] = (__bf16)a; v[1] = (__bf16)b;
  return __builtin_bit_cast(u32, v);
}
DEVI float dot2(u32 a, u32 b, float c) {
  return __builtin_amdgcn_fdot2_f32_bf16(__builtin_bit_cast(bf16v2, a), __builtin_bit_cast(bf16v2, b), c, false);
}
DEVI float rbf(float f) { return bf2f((u32)__builtin_bit_cast(u16, (__bf16)f)); }

template <bool SC1 = false> DEVI void glds16(const void* sbase_, u32 voff, u32 lds_) {
  u32 keep;
  const u64 sb_ = (u64)sbase_;
  const u64 sbase = ((u64)(u32)__builtin_amdgcn_readfirstlane((int)(sb_ >> 32)) << 32) |
                    (u64)(u32)__builtin_amdgcn_readfirstlane((int)sb_);
  const u32 lds = (u32)__builtin_amdgcn_readfirstlane((int)lds_);
  if constexpr (SC1)
    asm volatile("s_mov_b32 %0, m0\n\ts_mov_b32 m0, %3\n\ts_nop 0\n\tglobal_load_lds_dwordx4 %1, %2 sc1\n\ts_mov_b32 m0, %0"
                 : "=&s"(keep) : "v"(voff), "s"(sbase), "s"(lds) : "memory");
  else
    asm volatile("s_mov_b32 %0, m0\n\ts_mov_b32 m0, %3\n\ts_nop 0\n\tglobal_load_lds_dwordx4 %1, %2\n\ts_mov_b32 m0, %0"
                 : "=&s"(keep) : "v"(voff), "s"(sbase), "s"(lds) : "memory");
}
DEVI u32 lds_addr(const void* p) { return (u32)(size_t)(LDS_AS const char*)p; }
template <int N> DEVI void wait_vm() { asm volatile("s_waitcnt vmcnt(%0)" ::"n"(N) : "memory"); }
DEVI void wait_lgkm0() {
  __builtin_amdgcn_sched_barrier(0);
  __builtin_amdgcn_s_waitcnt(0xC07F);
  asm volatile("" ::: "memory");
  __builtin_amdgcn_sched_barrier(0);
}
DEVI void bar() { wait_lgkm0(); __builtin_amdgcn_s_barrier(); __builtin_amdgcn_sched_barrier(0); }

DEVI void st8(void* p, u32x2 v) {
  __hip_atomic_store((u64*)p, ((u64)v[1] << 32) | (u64)v[0], __ATOMIC_RELAXED, __HIP_MEMORY_SCOPE_AGENT);
}
DEVI void st16(void* p, u32x4 v) {
  asm volatile("global_store_dwordx4 %0, %1, off sc1\n\ts_nop 1" ::"v"(p), "v"(v) : "memory");
}
DEVI void publish(u32* cnt, bool stored) {
  if (stored) wait_vm<0>();
  bar();
  if (cnt != nullptr && threadIdx.x == 0) __hip_atomic_fetch_add(cnt, 1u, __ATOMIC_RELAXED, __HIP_MEMORY_SCOPE_AGENT);
}
DEVI void dep_wait(const u32* cnt) {
  int spins = 0;
  while (__hip_atomic_load(cnt, __ATOMIC_RELAXED, __HIP_MEMORY_SCOPE_AGENT) < 8u && ++spins < (1 << 21))
    __builtin_amdgcn_s_sleep(1);
}

template <int I> struct IC { static constexpr int v = I; };
template <int N, int I = 0, class F> DEVI void sfor(F&& f) {
  if constexpr (I < N) { f(IC<I>{}); sfor<N, I + 1>(f); }
}

template <int NRD, int NMM> DEVI void interleave() {
  constexpr int NP = NRD < NMM ? NRD : NMM;
#pragma unroll
  for (int i = 0; i < NP; ++i) {
    __builtin_amdgcn_sched_group_barrier(0x008, 1, 0);
    __builtin_amdgcn_sched_group_barrier(0x100, 1, 0);
  }
  if constexpr (NMM > NP) __builtin_amdgcn_sched_group_barrier(0x008, NMM - NP, 0);
  if constexpr (NRD > NP) __builtin_amdgcn_sched_group_barrier(0x100, NRD - NP, 0);
}
template <int NT, int NST, int NRD, int NMM, class Rd, class Mm> DEVI void ring_consumer(Rd&& rdfr, Mm&& mm) {
  bar();
  bar();
  rdfr(0, 0);
  int rd = 0;
#pragma unroll 1
  for (int kt = 0; kt < NT - 1; ++kt) {
    wait_lgkm0();
    rdfr(1, rd);
    mm(0);
    interleave<NRD, NMM>();
    rd = rd + 1 == NST ? 0 : rd + 1;
    bar();
    rdfr(0, rd);
    mm(1);
    interleave<NRD, NMM>();
  }
  wait_lgkm0();
  rdfr(1, rd);
  mm(0);
  interleave<NRD, NMM>();
  wait_lgkm0();
  mm(1);
}
template <int NT, int NST, int LA, int LB, int PRE, class LdA, class LdB>
DEVI void ring_loader(bool pre_done, LdA&& ldA, LdB&& ldB) {
  static_assert(PRE + 1 <= NST && PRE + 1 <= NT, "ring");
  constexpr int LPW = LA + LB;
  if (!pre_done) {
#pragma unroll 1
    for (int i = 0; i < PRE; ++i) ldB(i, i);
  }
  bar();
#pragma unroll 1
  for (int i = 0; i < PRE; ++i) ldA(i, i);
  wait_vm<LA * (PRE - 1)>();
  bar();
  ldA(PRE, PRE); ldB(PRE, PRE);
  int wr = PRE + 1 == NST ? 0 : PRE + 1;
  sfor<PRE - 1>([&](auto ic) {
    constexpr int kt = decltype(ic)::v;
    constexpr int nfull = (kt + 1 < NT - PRE ? kt + 1 : NT - PRE);
    wait_vm<LA * (PRE - 2 - kt) + LPW * nfull>();
    bar();
    if constexpr (kt + PRE + 1 < NT) {
      ldA(kt + PRE + 1, wr); ldB(kt + PRE + 1, wr);
      wr = wr + 1 == NST ? 0 : wr + 1;
    }
  });
#pragma unroll 1
  for (int kt = PRE - 1; kt < NT - 1 - PRE; ++kt) {
    wait_vm<LPW * (PRE - 1)>();
    bar();
    ldA(kt + PRE + 1, wr); ldB(kt + PRE + 1, wr);
    wr = wr + 1 == NST ? 0 : wr + 1;
  }
  constexpr int K0 = (PRE - 1 > NT - 1 - PRE) ? PRE - 1 : NT - 1 - PRE;
  sfor<NT - 1 - K0>([&](auto ic) {
    constexpr int kt = K0 + decltype(ic)::v;
    wait_vm<LPW * (NT - 2 - kt)>();
    bar();
  });
}

template <int CTRL> DEVI float dpp_f(float x) {
  return __builtin_bit_cast(float, __builtin_amdgcn_update_dpp(0, __builtin_bit_cast(int, x), CTRL, 0xF, 0xF, true));
}
DEVI float row16_sum(float x) {
  x += dpp_f<0xB1>(x);
  x += dpp_f<0x4E>(x);
  x += dpp_f<0x141>(x);
  x += dpp_f<0x140>(x);
  return x;
}

DEVI bf16x8 lds_b128(const char* p) { return *(const bf16x8*)p; }
DEVI bf16x8 lds_tr2(const char* p) {
  s16x4 a = __builtin_amdgcn_ds_read_tr16_b64_v4i16((LDS_AS s16x4*)p);
  s16x4 b = __builtin_amdgcn_ds_read_tr16_b64_v4i16((LDS_AS s16x4*)(p + 512));
  return __builtin_shufflevector(a, b, 0, 1, 2, 3, 4, 5, 6, 7);
}
DEVI f32x4 mfma(bf16x8 a, bf16x8 b, f32x4 c) { return __builtin_amdgcn_mfma_f32_16x16x32_bf16(a, b, c, 0, 0, 0); }

DEVI f32x2 gelu2(f32x2 x) {
  const f32x2 c1 = {-2.3022081985f, -2.3022081985f};
  const f32x2 c2 = {-0.10294324f, -0.10294324f};
  const f32x2 one = {1.0f, 1.0f};
  f32x2 a = x * (x * x * c2 + c1);
  f32x2 e = {__builtin_amdgcn_exp2f(a[0]), __builtin_amdgcn_exp2f(a[1])};
  f32x2 d = e + one;
  f32x2 r = {__builtin_amdgcn_rcpf(d[0]), __builtin_amdgcn_rcpf(d[1])};
  return x * r;
}
DEVI f32x2 unpk(u32 w) { return (f32x2){bflo(w), bfhi(w)}; }

DEVI f32x2 stat_fin(const f32x4& sm, const f32x4& sg, int lane) {
  const int j = lane & 3;
  float dg = j == 0 ? sg[0] : (j == 1 ? sg[1] : (j == 2 ? sg[2] : sg[3]));
  const float q = __shfl(dg, (lane & 15) + 16 * ((lane & 15) >> 2));
  const float mean = sm[0] * (1.0f / 512.0f);
  const float var = fmaxf(q * (1.0f / 512.0f) - mean * mean, 0.f);
  const float sd = rbf(__builtin_sqrtf(rbf(rbf(var) + 1e-5f)));
  return (f32x2){rbf(mean), 1.0f / sd};
}

struct Params {
  const u16 *wqkv, *wo, *w_in, *w_sp, *b_sp, *w_out;
  const u16 *z2t_in, *z1_in, *x1_in;
  u16 *h, *xout;
  const u16* xin;
  u16 *o, *x1, *z1, *z2t;
  u64* gran;
  u32* cnt;
  u32* zero_area;
  int zero_words, phases;
};
struct Ctx { int tid, lane, wid, rt, c8; u32 lb; };

constexpr int K1_AST = 80 * 128;
constexpr int K1_ST = K1_AST + 64 * 192 * 2;
constexpr int K1_NST = 4;
constexpr int K3_AST = 64 * 128;
constexpr int K3_ST = K3_AST + 64 * 192 * 2;
constexpr int K3_NST = 4;
constexpr int PJ_ST = 16384, PJ_NST = 7, PJ_PRE = 6, PJ_BASE = 40960;

DEVI void p1_ldB(const Params& p, const Ctx& c, int kt, int buf) {
  const int lw = c.wid - 4, lane = c.lane;
#pragma unroll
  for (int i = 0; i < 6; ++i) {
    int pi = lw * 6 + i, panel = pi >> 3, pp = pi & 7;
    int f = ((lane >> 4) & 1) | ((i & 1) << 1);
    int sc = ((((lane & 7) >> 1) ^ f) << 1) | (lane & 1);
    glds16(p.wqkv + (size_t)(kt * 64 + 8 * pp) * 1536 + panel * 512 + c.c8 * 64, (u32)((lane >> 3) * 3072 + sc * 16),
           c.lb + buf * K1_ST + K1_AST + panel * 8192 + pp * 1024);
  }
}
DEVI void p3_ldB(const Params& p, const Ctx& c, int kt, int buf) {
  const int lw = c.wid - 4, lane = c.lane;
  const int cbase = (c.c8 >> 2) * FH + (c.c8 & 3) * 192;
#pragma unroll
  for (int i = 0; i < 6; ++i) {
    int pi = lw * 6 + i, panel = pi >> 3, pp = pi & 7;
    int f = ((lane >> 4) & 1) | ((i & 1) << 1);
    int sc = ((((lane & 7) >> 1) ^ f) << 1) | (lane & 1);
    int col = cbase + panel * 64 + sc * 8;
    col = col <= F2 - 8 ? col : F2 - 8;
    glds16(p.w_in + (size_t)(kt * 64 + 8 * pp) * F2, (u32)((lane >> 3) * (F2 * 2) + col * 2),
           c.lb + buf * K3_ST + K3_AST + panel * 8192 + pp * 1024);
  }
}
DEVI void pj_ldB(const u16* W, int KW, const Ctx& c, int kt, int buf) {
  const int lw = c.wid - 4, lane = c.lane;
#pragma unroll
  for (int i = 0; i < 2; ++i) {
    int f = ((lane >> 4) & 1) | (i << 1);
    int sc = ((((lane & 7) >> 1) ^ f) << 1) | (lane & 1);
    int krow = kt * 64 + 8 * (lw * 2 + i) + (lane >> 3);
    krow = krow < KW ? krow : KW - 1;
    glds16(W + c.c8 * 64, (u32)(krow * (D_ * 2) + sc * 16), c.lb + PJ_BASE + buf * PJ_ST + 8192 + (lw * 2 + i) * 1024);
  }
}
DEVI void next_pre(int next, const Params& p, const Ctx& c) {
  if (next == 5) {
#pragma unroll 1
    for (int i = 0; i < PJ_PRE; ++i) pj_ldB(p.w_out, FH, c, i, i);
  } else if (next == 2) {
#pragma unroll 1
    for (int i = 0; i < PJ_PRE; ++i) pj_ldB(p.wo, D_, c, i, i);
  } else if (next == 1) {
    p1_ldB(p, c, 0, 0); p1_ldB(p, c, 1, 1);
  } else if (next == 3) {
    p3_ldB(p, c, 0, 0); p3_ldB(p, c, 1, 1);
  }
}

DEVI void phase1(char* smem, const Params& p, const Ctx& c, const u32* dep0, const u32* dep1, u32* sig,
                 bool pre_done, int next) {
  f32x2* sst = (f32x2*)(smem + K1_NST * K1_ST);
  const u16* __restrict__ x = p.xin;
  const int tid = c.tid, lane = c.lane, wid = c.wid, hg = c.c8;
  const int r0 = c.rt * 64;
  const u32 lb = c.lb;
  const int hw = wid & 3, mh = wid >> 2;
  const int l15 = lane & 15, g4 = lane >> 4;
  constexpr int QH = 1152, KH = 1408, HSZ = 2 * QH + 4 * KH;
  if (wid >= 4) {
    const int lw = wid - 4;
    const int ap0 = lw < 2 ? lw * 3 : 6 + (lw - 2) * 2;
    u32 avoff[3];
#pragma unroll
    for (int i = 0; i < 3; ++i) {
      int gr = r0 - 16 + 8 * (ap0 + i) + (lane >> 3); gr = gr < 0 ? 0 : (gr > S_ - 1 ? S_ - 1 : gr);
      avoff[i] = (u32)(gr * (D_ * 2) + (((lane & 7) ^ (lane >> 3)) << 4));
    }
    auto ldB = [&](int kt, int buf) { p1_ldB(p, c, kt, buf); };
    auto ldA3 = [&](int kt, int buf) {
#pragma unroll
      for (int i = 0; i < 3; ++i) glds16<true>(x + kt * 64, avoff[i], lb + buf * K1_ST + (ap0 + i) * 1024);
    };
    auto ldA2 = [&](int kt, int buf) {
#pragma unroll
      for (int i = 0; i < 2; ++i) glds16<true>(x + kt * 64, avoff[i], lb + buf * K1_ST + (ap0 + i) * 1024);
    };
    if (lw < 2) ring_loader<8, K1_NST, 3, 6, 2>(pre_done, ldA3, ldB);
    else ring_loader<8, K1_NST, 2, 6, 2>(pre_done, ldA2, ldB);
    for (int i = blockIdx.x * 256 + (tid - 256); i < p.zero_words; i += 256 * 256) p.zero_area[i] = 0u;
    bar();
    next_pre(next, p, c);
    if (p.zero_words != 0) {
      const u32 junk = lb + 40960 + lw * 1024;
      const u32 vo = (u32)(lane * 16);
      const int b = blockIdx.x, bc = b < 172 ? b : 171;
      glds16((const char*)p.wo + (size_t)b * 2048 + (lw & 1) * 1024, vo, junk);
      glds16((const char*)p.w_in + (size_t)bc * 8192 + lw * 2048, vo, junk);
      glds16((const char*)p.w_in + (size_t)bc * 8192 + lw * 2048 + 1024, vo, junk);
#pragma unroll 1
      for (int i = lw; i < 32; i += 4) glds16((const char*)p.w_sp + (size_t)b * 32768 + i * 1024, vo, junk);
      glds16((const char*)p.w_out + (size_t)bc * 4096 + lw * 1024, vo, junk);
    }
  } else {
    if (wid == 0 && dep0 != nullptr) { dep_wait(dep0); if (dep1 != nullptr) dep_wait(dep1); }
    f32x4 acc[14], cs[3];
#pragma unroll
    for (int i = 0; i < 14; ++i) acc[i] = (f32x4){0.f, 0.f, 0.f, 0.f};
#pragma unroll
    for (int i = 0; i < 3; ++i) cs[i] = (f32x4){0.f, 0.f, 0.f, 0.f};
    const bf16x8 ones = {0x3F80, 0x3F80, 0x3F80, 0x3F80, 0x3F80, 0x3F80, 0x3F80, 0x3F80};
    int aoff[2];
#pragma unroll
    for (int ks = 0; ks < 2; ++ks) aoff[ks] = l15 * 128 + (((4 * ks + g4) ^ (l15 & 7)) << 4);
    const int fl = ((lane >> 3) & 1) | (((lane >> 4) & 1) << 1);
    const int boff = K1_AST + (8 * g4 + (l15 >> 2)) * 128 + 8 * (lane & 3) + ((hw ^ fl) << 5);
    f32x4 sm = {0.f, 0.f, 0.f, 0.f}, sg = sm, sm4 = sm, sg4 = sm;
    bf16x8 af[2][5], bf[2][3], sf[2];
    auto rdfr = [&](int ks, int buf) {
      const char* sb = smem + buf * K1_ST;
#pragma unroll
      for (int pn = 0; pn < 3; ++pn) bf[ks][pn] = lds_tr2(sb + boff + pn * 8192 + ks * 4096);
#pragma unroll
      for (int mt = 0; mt < 5; ++mt) af[ks][mt] = lds_b128(sb + mt * 2048 + aoff[ks]);
      sf[ks] = lds_b128(sb + hw * 2048 + aoff[ks]);
    };
    auto mm = [&](int ks) {
#pragma unroll
      for (int pn = 0; pn < 3; ++pn) cs[pn] = mfma(bf[ks][pn], ones, cs[pn]);
#pragma unroll
      for (int mt = 0; mt < 5; ++mt) {
        if (mt > 0) acc[mt - 1] = mfma(bf[ks][0], af[ks][mt], acc[mt - 1]);
        acc[4 + mt] = mfma(bf[ks][1], af[ks][mt], acc[4 + mt]);
        acc[9 + mt] = mfma(bf[ks][2], af[ks][mt], acc[9 + mt]);
      }
      sm = mfma(ones, sf[ks], sm); sg = mfma(sf[ks], sf[ks], sg);
      sm4 = mfma(ones, af[ks][4], sm4); sg4 = mfma(af[ks][4], af[ks][4], sg4);
    };
    ring_consumer<8, K1_NST, 12, 21>(rdfr, mm);
    {
      const f32x2 st = stat_fin(sm, sg, lane), st4 = stat_fin(sm4, sg4, lane);
      if (lane < 16) { sst[hw * 16 + lane] = st; if (hw == 0) sst[64 + lane] = st4; }
    }
    bar();
    char* sw = smem + hw * HSZ;
    char* sq = sw; char* sk = sw + 2 * QH; char* sv = sk + 2 * KH;
    const int half = g4 >> 1, o8 = (g4 & 1) * 8;
#pragma unroll
    for (int mt = 0; mt < 5; ++mt) {
      const f32x2 ms = sst[mt * 16 + l15];
      auto put = [&](char* base, int hs, int row, const f32x4& a, const f32x4& c) {
        u32x2 pq = {pack2((a[0] - ms[0] * c[0]) * ms[1], (a[1] - ms[0] * c[1]) * ms[1]),
                    pack2((a[2] - ms[0] * c[2]) * ms[1], (a[3] - ms[0] * c[3]) * ms[1])};
        *(u32x2*)(base + half * hs + row * 16 + o8) = pq;
      };
      if (mt > 0) put(sq, QH, (mt - 1) * 16 + l15, acc[mt - 1], cs[0]);
      put(sk, KH, mt * 16 + l15, acc[4 + mt], cs[1]);
      put(sv, KH, mt * 16 + l15, acc[9 + mt], cs[2]);
    }
  }
  bar();
  {
    const int hf = lane & 1, tl = mh * 32 + (lane >> 1);
    const int t = r0 + tl;
    const char* sq = smem + hw * HSZ + hf * QH;
    const char* sk = smem + hw * HSZ + 2 * QH + hf * KH;
    const char* sv = sk + 2 * KH;
    u32x4 q0 = *(const u32x4*)(sq + tl * 16);
    float lg[8];
    float mx = -3.0e38f;
#pragma unroll
    for (int j = 0; j < 8; ++j) {
      u32x4 k0 = *(const u32x4*)(sk + (tl + 16 - 2 * j) * 16);
      float d = 0.f;
#pragma unroll
      for (int e = 0; e < 4; ++e) d = dot2(q0[e], k0[e], d);
      d += dpp_f<0xB1>(d);
      d = rbf(d) * 0.25f;
      d = (t - 2 * j >= 0) ? d : -1.0e9f;
      lg[j] = d;
      mx = fmaxf(mx, d);
    }
    float sum = 0.f;
#pragma unroll
    for (int j = 0; j < 8; ++j) { lg[j] = __expf(lg[j] - mx); sum += lg[j]; }
    const float rs = 1.0f / sum;
    float ov[8];
#pragma unroll
    for (int e = 0; e < 8; ++e) ov[e] = 0.f;
#pragma unroll
    for (int jp = 0; jp < 4; ++jp) {
      const u32 pp = pack2(lg[2 * jp] * rs, lg[2 * jp + 1] * rs);
      int ea = tl + 16 - 4 * jp;
      u32x4 va = *(const u32x4*)(sv + ea * 16), vb = *(const u32x4*)(sv + (ea - 2) * 16);
#pragma unroll
      for (int e = 0; e < 4; ++e) {
        u32 lo = __builtin_amdgcn_perm(vb[e], va[e], 0x05040100u);
        u32 hi = __builtin_amdgcn_perm(vb[e], va[e], 0x07060302u);
        ov[2 * e] = dot2(lo, pp, ov[2 * e]);
        ov[2 * e + 1] = dot2(hi, pp, ov[2 * e + 1]);
      }
    }
    u32x4 w0;
#pragma unroll
    for (int e = 0; e < 4; ++e) w0[e] = pack2(ov[2 * e], ov[2 * e + 1]);
    st16(p.o + (size_t)t * D_ + hg * 64 + hw * 16 + hf * 8, w0);
  }
  publish(sig, true);
}

template <int LDA, int NT, int KW>
DEVI void phase_proj(char* smem, const Ctx& c, const u16* __restrict__ A, const u16* __restrict__ W,
                     const u16* __restrict__ res, u16* __restrict__ out, const u32* dep, u32* sig,
                     const Params& p, bool pre_done, int next) {
  constexpr int ST = PJ_ST, NST = PJ_NST;
  const int lane = c.lane, wid = c.wid;
  const int r0 = c.rt * 64, c0 = c.c8 * 64;
  const u32 lb = c.lb;
  if (wid >= 4) {
    const int lw = wid - 4;
    const u32 avoff = (u32)((lane >> 3) * (LDA * 2) + (((lane & 7) ^ (lane >> 3)) << 4));
    ring_loader<NT, NST, 2, 2, PJ_PRE>(
        pre_done,
        [&](int kt, int buf) {
#pragma unroll
          for (int i = 0; i < 2; ++i)
            glds16<true>(A + (size_t)(r0 + 8 * (lw * 2 + i)) * LDA + kt * 64, avoff, lb + PJ_BASE + buf * ST + (lw * 2 + i) * 1024);
        },
        [&](int kt, int buf) { pj_ldB(W, KW, c, kt, buf); });
    bar();
    next_pre(next, p, c);
  } else {
    const int l15 = lane & 15, g4 = lane >> 4;
    const int wm = wid >> 1, wn = wid & 1;
    u32x2 rv[2][2];
#pragma unroll
    for (int i = 0; i < 2; ++i)
#pragma unroll
      for (int j = 0; j < 2; ++j)
        rv[i][j] = *(const u32x2*)(res + (size_t)(r0 + wm * 32 + i * 16 + l15) * D_ + c0 + wn * 32 + j * 16 + 4 * g4);
    if (wid == 0 && dep != nullptr) dep_wait(dep);
    f32x4 acc[2][2];
#pragma unroll
    for (int i = 0; i < 2; ++i)
#pragma unroll
      for (int j = 0; j < 2; ++j) acc[i][j] = (f32x4){0.f, 0.f, 0.f, 0.f};
    int aoff[2], boff[2];
    const int fl = ((lane >> 3) & 1) | (((lane >> 4) & 1) << 1);
#pragma unroll
    for (int ks = 0; ks < 2; ++ks) aoff[ks] = (wm * 32 + l15) * 128 + (((4 * ks + g4) ^ (l15 & 7)) << 4);
#pragma unroll
    for (int j = 0; j < 2; ++j) boff[j] = 8192 + (8 * g4 + (l15 >> 2)) * 128 + 8 * (lane & 3) + (((wn * 2 + j) ^ fl) << 5);
    bf16x8 af[2][2], bf[2][2];
    ring_consumer<NT, NST, 6, 4>(
        [&](int ks, int buf) {
          const char* sb = smem + PJ_BASE + buf * ST;
#pragma unroll
          for (int i = 0; i < 2; ++i) af[ks][i] = lds_b128(sb + i * 2048 + aoff[ks]);
#pragma unroll
          for (int j = 0; j < 2; ++j) bf[ks][j] = lds_tr2(sb + boff[j] + ks * 4096);
        },
        [&](int ks) {
#pragma unroll
          for (int i = 0; i < 2; ++i)
#pragma unroll
            for (int j = 0; j < 2; ++j) acc[i][j] = mfma(bf[ks][j], af[ks][i], acc[i][j]);
        });
    bar();
#pragma unroll
    for (int i = 0; i < 2; ++i)
#pragma unroll
      for (int j = 0; j < 2; ++j) {
        u32x2 r = rv[i][j], ov;
        ov[0] = pack2(bflo(r[0]) + rbf(acc[i][j][0]), bfhi(r[0]) + rbf(acc[i][j][1]));
        ov[1] = pack2(bflo(r[1]) + rbf(acc[i][j][2]), bfhi(r[1]) + rbf(acc[i][j][3]));
        st8(out + (size_t)(r0 + wm * 32 + i * 16 + l15) * D_ + c0 + wn * 32 + j * 16 + 4 * g4, ov);
      }
  }
  publish(sig, wid < 4);
}

constexpr int K3E_ACC = 0;
constexpr int K3E_ST = 36864;
constexpr int K3E_FIN = 38912;
constexpr int K3E_OUT = 40960;

template <bool Z2>
DEVI void k3_epi(char* smem, f32x4 (&acc)[2][3], const f32x4 (&cs)[3], u16* __restrict__ z1, u16* __restrict__ z2t,
                 u64* __restrict__ gran, int rt, int ct, int lane, int wn, int mt0, int tid) {
  const int r0 = rt * 64;
  const int l15 = lane & 15, g4 = lane >> 4;
  const f32x2* sst = (const f32x2*)(smem + K3_NST * K3_ST);
  const int u0 = ct * 12 + wn * 3;
  int nv = 43 - u0; nv = nv < 0 ? 0 : (nv > 3 ? 3 : nv);
  const int ncols = (ct < 3 ? 12 : 7) * 16;
  u32 zp[2][3][2];
  auto act = [&](int i) {
    f32x2 mn[2], rs[2];
    if constexpr (Z2) {
      const f32x4 a = *(const f32x4*)(sst + (mt0 + i) * 16 + 4 * g4), b = *(const f32x4*)(sst + (mt0 + i) * 16 + 4 * g4 + 2);
      mn[0] = (f32x2){a[0], a[2]}; rs[0] = (f32x2){a[1], a[3]};
      mn[1] = (f32x2){b[0], b[2]}; rs[1] = (f32x2){b[1], b[3]};
    } else {
      const f32x2 ms = sst[(mt0 + i) * 16 + l15];
      mn[0] = mn[1] = (f32x2){ms[0], ms[0]}; rs[0] = rs[1] = (f32x2){ms[1], ms[1]};
    }
#pragma unroll
    for (int j = 0; j < 3; ++j)
#pragma unroll
      for (int hh = 0; hh < 2; ++hh) {
        f32x2 a = {acc[i][j][2 * hh], acc[i][j][2 * hh + 1]}, c = {cs[j][2 * hh], cs[j][2 * hh + 1]};
        f32x2 y = (a - mn[hh] * c) * rs[hh];
        f32x2 g = gelu2(unpk(pack2(y[0], y[1])));
        zp[i][j][hh] = pack2(g[0], g[1]);
      }
  };
  if constexpr (!Z2) {
    act(0); act(1);
    char* img = smem + K3E_OUT;
#pragma unroll
    for (int i = 0; i < 2; ++i)
#pragma unroll
      for (int j = 0; j < 3; ++j)
        *(u32x2*)(img + ((mt0 + i) * 16 + l15) * 400 + (wn * 48 + j * 16 + 4 * g4) * 2) = (u32x2){zp[i][j][0], zp[i][j][1]};
    bar();
#pragma unroll
    for (int it = 0; it < 3; ++it) {
      int c = tid + it * 512, row = c / 24, ch = c - row * 24;
      if (ch * 8 < ncols)
        *(u32x4*)(z1 + (size_t)(r0 + row) * FH + ct * 192 + ch * 8) = *(const u32x4*)(img + row * 400 + ch * 16);
    }
  } else {
    f32x2* st = (f32x2*)(smem + K3E_ST);
    f32x2* fin = (f32x2*)(smem + K3E_FIN);
    u64* gbase = gran + (size_t)(rt * 4) * 128;
#pragma unroll
    for (int i = 0; i < 2; ++i) {
      act(i);
      float sv[8];
#pragma unroll
      for (int hh = 0; hh < 2; ++hh) {
        f32x2 s = {0.f, 0.f}, q = {0.f, 0.f};
#pragma unroll
        for (int j = 0; j < 3; ++j) {
          const f32x2 z = unpk(j < nv ? zp[i][j][hh] : 0u);
          s += z; q += z * z;
        }
        sv[hh * 4 + 0] = s[0]; sv[hh * 4 + 1] = q[0]; sv[hh * 4 + 2] = s[1]; sv[hh * 4 + 3] = q[1];
      }
#pragma unroll
      for (int k = 0; k < 8; ++k) sv[k] += dpp_f<0xB1>(sv[k]);
#pragma unroll
      for (int k = 0; k < 8; ++k) sv[k] += dpp_f<0x4E>(sv[k]);
#pragma unroll
      for (int k = 0; k < 8; ++k) sv[k] += dpp_f<0x141>(sv[k]);
#pragma unroll
      for (int k = 0; k < 8; ++k) sv[k] += dpp_f<0x140>(sv[k]);
      if (l15 == 0) {
        *(f32x4*)(st + wn * 64 + (mt0 + i) * 16 + 4 * g4) = (f32x4){sv[0], sv[1], sv[2], sv[3]};
        *(f32x4*)(st + wn * 64 + (mt0 + i) * 16 + 4 * g4 + 2) = (f32x4){sv[4], sv[5], sv[6], sv[7]};
      }
      bar();
      if (tid < 32) {
        const int row = (tid >> 4) * 32 + i * 16 + (tid & 15);
        const f32x2 pr = st[row] + st[64 + row] + st[128 + row] + st[192 + row];
        __hip_atomic_store(gbase + ct * 128 + row * 2, (1ull << 32) | (u64)__float_as_uint(pr[0]), __ATOMIC_RELAXED, __HIP_MEMORY_SCOPE_AGENT);
        __hip_atomic_store(gbase + ct * 128 + row * 2 + 1, (1ull << 32) | (u64)__float_as_uint(pr[1]), __ATOMIC_RELAXED, __HIP_MEMORY_SCOPE_AGENT);
      }
    }
    if (tid < 64) {
      float ts, tq;
      int spins = 0;
      bool ok;
      do {
        ok = true; ts = 0.f; tq = 0.f;
#pragma unroll
        for (int c = 0; c < 4; ++c) {
          u64 a = __hip_atomic_load(gbase + c * 128 + tid * 2, __ATOMIC_RELAXED, __HIP_MEMORY_SCOPE_AGENT);
          u64 b = __hip_atomic_load(gbase + c * 128 + tid * 2 + 1, __ATOMIC_RELAXED, __HIP_MEMORY_SCOPE_AGENT);
          ok = ok && (a >> 32) == 1ull && (b >> 32) == 1ull;
          ts += __uint_as_float((u32)a); tq += __uint_as_float((u32)b);
        }
      } while (!ok && ++spins < (1 << 20));
      const float tmean = ts * (1.0f / 688.0f);
      const float vb = rbf(fmaxf(tq * (1.0f / 688.0f) - tmean * tmean, 0.f));
      const float sd = rbf(__builtin_sqrtf(rbf(vb + 1e-5f)));
      fin[tid] = (f32x2){rbf(tmean), 1.0f / sd};
    }
    bar();
    char* img = smem + K3E_OUT;
#pragma unroll
    for (int i = 0; i < 2; ++i) {
      const int row = (mt0 + i) * 16 + 4 * g4;
      const f32x4 a = *(const f32x4*)(fin + row), b = *(const f32x4*)(fin + row + 2);
      const f32x2 mn0 = {a[0], a[2]}, rs0 = {a[1], a[3]}, mn1 = {b[0], b[2]}, rs1 = {b[1], b[3]};
#pragma unroll
      for (int j = 0; j < 3; ++j) {
        f32x2 d0 = unpk(zp[i][j][0]) - mn0, d1 = unpk(zp[i][j][1]) - mn1;
        d0 = unpk(pack2(d0[0], d0[1])) * rs0; d1 = unpk(pack2(d1[0], d1[1])) * rs1;
        *(u32x2*)(img + (wn * 48 + j * 16 + l15) * 144 + row * 2) = (u32x2){pack2(d0[0], d0[1]), pack2(d1[0], d1[1])};
      }
    }
    bar();
#pragma unroll
    for (int it = 0; it < 3; ++it) {
      int c = tid + it * 512, f = c >> 3, ch = c & 7;
      if (f < ncols)
        *(u32x4*)(z2t + (size_t)(ct * 192 + f) * S_ + r0 + ch * 8) = *(const u32x4*)(img + f * 144 + ch * 16);
    }
  }
}

template <bool Z2>
DEVI void k3_compute(char* smem, u16* __restrict__ z1, u16* __restrict__ z2t,
                     u64* __restrict__ gran, int rt, int ct, int lane, int wn, int tid) {
  f32x2* sst = (f32x2*)(smem + K3_NST * K3_ST);
  f32x4 acc[2][3], acc2[2][3], cs[3];
#pragma unroll
  for (int i = 0; i < 2; ++i)
#pragma unroll
    for (int j = 0; j < 3; ++j) { acc[i][j] = (f32x4){0.f, 0.f, 0.f, 0.f}; acc2[i][j] = acc[i][j]; }
#pragma unroll
  for (int j = 0; j < 3; ++j) cs[j] = (f32x4){0.f, 0.f, 0.f, 0.f};
  const bf16x8 ones = {0x3F80, 0x3F80, 0x3F80, 0x3F80, 0x3F80, 0x3F80, 0x3F80, 0x3F80};
  const int l15 = lane & 15, g4 = lane >> 4;
  int aoff[2], boff[3];
#pragma unroll
  for (int ks = 0; ks < 2; ++ks) aoff[ks] = l15 * 128 + (((4 * ks + g4) ^ (l15 & 7)) << 4);
  const int fl = ((lane >> 3) & 1) | (((lane >> 4) & 1) << 1);
#pragma unroll
  for (int j = 0; j < 3; ++j) {
    int nt = wn * 3 + j;
    boff[j] = K3_AST + (nt >> 2) * 8192 + (8 * g4 + (l15 >> 2)) * 128 + 8 * (lane & 3) + (((nt & 3) ^ fl) << 5);
  }
  f32x4 sm = {0.f, 0.f, 0.f, 0.f}, sg = sm;
  bf16x8 af[2][4], bf[2][3], sf[2];
  auto rdfr = [&](int ks, int buf) {
    const char* sb = smem + buf * K3_ST;
#pragma unroll
    for (int j = 0; j < 3; ++j) bf[ks][j] = lds_tr2(sb + boff[j] + ks * 4096);
#pragma unroll
    for (int mt = 0; mt < 4; ++mt) af[ks][mt] = lds_b128(sb + mt * 2048 + aoff[ks]);
    sf[ks] = lds_b128(sb + wn * 2048 + aoff[ks]);
  };
  auto mm = [&](int ks) {
#pragma unroll
    for (int j = 0; j < 3; ++j) {
      if constexpr (Z2) cs[j] = mfma(ones, bf[ks][j], cs[j]);
      else cs[j] = mfma(bf[ks][j], ones, cs[j]);
    }
#pragma unroll
    for (int mt = 0; mt < 2; ++mt)
#pragma unroll
      for (int j = 0; j < 3; ++j) {
        if constexpr (Z2) { acc[mt][j] = mfma(af[ks][mt], bf[ks][j], acc[mt][j]); acc2[mt][j] = mfma(af[ks][2 + mt], bf[ks][j], acc2[mt][j]); }
        else { acc[mt][j] = mfma(bf[ks][j], af[ks][mt], acc[mt][j]); acc2[mt][j] = mfma(bf[ks][j], af[ks][2 + mt], acc2[mt][j]); }
      }
    sm = mfma(ones, sf[ks], sm); sg = mfma(sf[ks], sf[ks], sg);
  };
  ring_consumer<8, K3_NST, 11, 17>(rdfr, mm);
  bar();
  {
    const f32x2 st = stat_fin(sm, sg, lane);
    if (lane < 16) sst[wn * 16 + lane] = st;
    f32x4* ho = (f32x4*)(smem + K3E_ACC) + wn * 9 * 64 + lane;
#pragma unroll
    for (int i = 0; i < 2; ++i)
#pragma unroll
      for (int j = 0; j < 3; ++j) ho[(i * 3 + j) * 64] = acc2[i][j];
#pragma unroll
    for (int j = 0; j < 3; ++j) ho[(6 + j) * 64] = cs[j];
  }
  bar();
  k3_epi<Z2>(smem, acc, cs, z1, z2t, gran, rt, ct, lane, wn, 0, tid);
}

template <bool Z2>
DEVI void k3_helper(char* smem, u16* __restrict__ z1, u16* __restrict__ z2t,
                    u64* __restrict__ gran, int rt, int ct, int lane, int wn, int tid) {
  bar();
  bar();
  f32x4 acc[2][3], cs[3];
  const f32x4* hi = (const f32x4*)(smem + K3E_ACC) + wn * 9 * 64 + lane;
#pragma unroll
  for (int i = 0; i < 2; ++i)
#pragma unroll
    for (int j = 0; j < 3; ++j) acc[i][j] = hi[(i * 3 + j) * 64];
#pragma unroll
  for (int j = 0; j < 3; ++j) cs[j] = hi[(6 + j) * 64];
  k3_epi<Z2>(smem, acc, cs, z1, z2t, gran, rt, ct, lane, wn, 2, tid);
}

DEVI void phase3(char* smem, const Params& p, const Ctx& c, const u32* dep, bool pre_done) {
  const u16* __restrict__ x = p.x1;
  const int tid = c.tid, lane = c.lane, wid = c.wid, rt = c.rt, c8 = c.c8;
  const int r0 = rt * 64;
  const u32 lb = c.lb;
  if (wid >= 4) {
    const int lw = wid - 4;
    const u32 avoff = (u32)((lane >> 3) * (D_ * 2) + (((lane & 7) ^ (lane >> 3)) << 4));
    ring_loader<8, K3_NST, 2, 6, 2>(
        pre_done,
        [&](int kt, int buf) {
#pragma unroll
          for (int i = 0; i < 2; ++i)
            glds16<true>(x + (size_t)(r0 + 8 * (lw * 2 + i)) * D_ + kt * 64, avoff, lb + buf * K3_ST + (lw * 2 + i) * 1024);
        },
        [&](int kt, int buf) { p3_ldB(p, c, kt, buf); });
    if (c8 < 4) k3_helper<false>(smem, p.z1, p.z2t, p.gran, rt, c8, lane, lw, tid);
    else k3_helper<true>(smem, p.z1, p.z2t, p.gran, rt, c8 - 4, lane, lw, tid);
  } else {
    if (wid == 0 && dep != nullptr) dep_wait(dep);
    if (c8 < 4) k3_compute<false>(smem, p.z1, p.z2t, p.gran, rt, c8, lane, wid, tid);
    else k3_compute<true>(smem, p.z1, p.z2t, p.gran, rt, c8 - 4, lane, wid, tid);
  }
}

constexpr int K4_ST = (64 + 96) * 256;
constexpr int K4_NST = 4;
constexpr int K4_NT = 16;

DEVI void phase4(char* smem, const Params& p, const Ctx& c, u32* sig, int next, const Ctx& cn) {
  const int lane = c.lane, wid = c.wid, nt0 = c.c8;
  const int r0 = c.rt * 64;
  const int u0 = nt0 * 6 < 37 ? nt0 * 6 : 37;
  const int f0 = u0 * 16;
  if (wid >= 4) {
    const int lw = wid - 4;
    const u16* asrc = p.w_sp + (size_t)(r0 + 16 * lw) * S_;
    const u16* bsrc = p.z2t_in + (size_t)(f0 + 24 * lw) * S_;
    const u32 lb = c.lb;
    u32 voff[4];
#pragma unroll
    for (int i = 0; i < 4; ++i) voff[i] = (u32)((lane >> 4) * (S_ * 2) + (((lane & 15) ^ (4 * i + (lane >> 4))) << 4));
    const int bsh = (lw & 1) * 2;
    ring_loader<K4_NT, K4_NST, 4, 6, K4_NST - 1>(
        false,
        [&](int kt, int buf) {
#pragma unroll
          for (int i = 0; i < 4; ++i) glds16(asrc + (size_t)(4 * i) * S_ + kt * 128, voff[i], lb + buf * K4_ST + (lw * 4 + i) * 1024);
        },
        [&](int kt, int buf) {
#pragma unroll
          for (int i = 0; i < 6; ++i) {
            const u32 v = bsh ? voff[(i + 2) & 3] : voff[i & 3];
            glds16(bsrc + (size_t)(4 * i) * S_ + kt * 128, v, lb + buf * K4_ST + 16384 + (lw * 6 + i) * 1024);
          }
        });
    bar();
    next_pre(next, p, cn);
  } else {
    const int l15 = lane & 15, g4 = lane >> 4;
    const int wm = wid >> 1, wn = wid & 1;
    u32x2 zv[2][3];
    float bs[2];
#pragma unroll
    for (int i = 0; i < 2; ++i) {
      bs[i] = bf2f(p.b_sp[r0 + wm * 32 + i * 16 + l15]);
#pragma unroll
      for (int j = 0; j < 3; ++j)
        zv[i][j] = *(const u32x2*)(p.z1_in + (size_t)(r0 + wm * 32 + i * 16 + l15) * FH + f0 + wn * 48 + j * 16 + 4 * g4);
    }
    f32x4 acc[2][3];
#pragma unroll
    for (int i = 0; i < 2; ++i)
#pragma unroll
      for (int j = 0; j < 3; ++j) acc[i][j] = (f32x4){0.f, 0.f, 0.f, 0.f};
    int xo[4];
#pragma unroll
    for (int k4 = 0; k4 < 4; ++k4) xo[k4] = l15 * 256 + (((4 * k4 + g4) ^ l15) << 4);
    const int abase = wm * 32 * 256, bbase = 16384 + wn * 48 * 256;
    bf16x8 af[2][2][2], bf[2][2][3];
    ring_consumer<K4_NT, K4_NST, 10, 12>(
        [&](int hs, int buf) {
          const char* sb = smem + buf * K4_ST;
#pragma unroll
          for (int ks = 0; ks < 2; ++ks) {
#pragma unroll
            for (int i = 0; i < 2; ++i) af[hs][ks][i] = lds_b128(sb + abase + i * 4096 + xo[2 * hs + ks]);
#pragma unroll
            for (int j = 0; j < 3; ++j) bf[hs][ks][j] = lds_b128(sb + bbase + j * 4096 + xo[2 * hs + ks]);
          }
        },
        [&](int hs) {
#pragma unroll
          for (int ks = 0; ks < 2; ++ks)
#pragma unroll
            for (int i = 0; i < 2; ++i)
#pragma unroll
              for (int j = 0; j < 3; ++j) acc[i][j] = mfma(bf[hs][ks][j], af[hs][ks][i], acc[i][j]);
        });
    bar();
    const int umin = nt0 * 6;
#pragma unroll
    for (int i = 0; i < 2; ++i) {
      u16* hp = p.h + (size_t)(r0 + wm * 32 + i * 16 + l15) * FHP;
#pragma unroll
      for (int j = 0; j < 3; ++j) {
        int u = u0 + wn * 3 + j;
        if (u >= umin) {
          u32x2 z = zv[i][j], ov;
          float g0 = rbf(rbf(acc[i][j][0]) + bs[i]), g1 = rbf(rbf(acc[i][j][1]) + bs[i]);
          float g2 = rbf(rbf(acc[i][j][2]) + bs[i]), g3 = rbf(rbf(acc[i][j][3]) + bs[i]);
          ov[0] = pack2(bflo(z[0]) * g0, bfhi(z[0]) * g1);
          ov[1] = pack2(bflo(z[1]) * g2, bfhi(z[1]) * g3);
          st8(hp + u * 16 + 4 * g4, ov);
        }
      }
      if (nt0 == 7 && wn == 1) st8(hp + FH + 4 * g4, (u32x2){0u, 0u});
    }
  }
  publish(sig, wid < 4);
}

__global__ __launch_bounds__(512, 2) void fused_phases(const Params p) {
  __shared__ __attribute__((aligned(16))) char smem[163840];
  Ctx c;
  c.tid = threadIdx.x; c.lane = c.tid & 63;
  c.wid = __builtin_amdgcn_readfirstlane(c.tid >> 6);
  const int bid = blockIdx.x, xcd = bid & 7, idx = bid >> 3;
  c.rt = (xcd >> 1) * 8 + (idx & 7);
  c.c8 = (xcd & 1) * 4 + (idx >> 3);
  c.lb = lds_addr(smem);
  const int ph = p.phases;
  u32* cnt = p.cnt + c.rt * 32;
  if (ph & 1) { phase4(smem, p, c, cnt, 5, c); }
  if (ph & 2) { phase_proj<FHP, 11, FH>(smem, c, p.h, p.w_out, p.x1_in, p.xout, cnt, (ph & 4) ? cnt + 1024 : nullptr, p, true, (ph & 4) ? 1 : 0); }
  if (ph & 4) {
    phase1(smem, p, c, (ph & 2) ? cnt + 1024 : nullptr, ((ph & 2) && c.rt > 0) ? cnt + 1024 - 32 : nullptr,
           (ph & 8) ? cnt + 2048 : nullptr, (ph & 2) != 0, (ph & 8) ? 2 : 0);
  }
  if (ph & 8) { phase_proj<D_, 8, D_>(smem, c, p.o, p.wo, p.xin, p.x1, (ph & 4) ? cnt + 2048 : nullptr, cnt + 3072, p, (ph & 4) != 0, 3); }
  if (ph & 16) { phase3(smem, p, c, cnt + 3072, true); }
}

extern "C" void kernel_launch(void* const* d_in, const int* in_sizes, int n_in, void* d_out, int out_size,
                              void* d_ws, size_t ws_size, hipStream_t stream) {
  char* ws = (char*)d_ws;
  constexpr size_t MB = 1 << 20;
  u64* gran = (u64*)ws;
  u32* cnt = (u32*)(ws + 256 * 1024);
  const int zero_words = (256 * 1024 + 3 * 16384) / 4;
  auto buf = [&](int i) { return (u16*)(ws + (1 + 3 * i) * MB); };
  u16 *o[2] = {buf(0), buf(1)}, *x1[2] = {buf(2), buf(3)}, *z1[2] = {buf(4), buf(5)}, *z2t[2] = {buf(6), buf(7)};
  u16 *h[2] = {buf(8), buf(9)}, *x2 = buf(10);

  Params p;
  p.wqkv = (const u16*)d_in[1]; p.wo = (const u16*)d_in[2]; p.w_in = (const u16*)d_in[4];
  p.w_sp = (const u16*)d_in[5]; p.b_sp = (const u16*)d_in[6]; p.w_out = (const u16*)d_in[7];
  p.zero_area = (u32*)ws;
  p.z2t_in = z2t[0]; p.z1_in = z1[0]; p.x1_in = x1[0]; p.h = h[0]; p.xout = x2;
  p.xin = (const u16*)d_in[0]; p.o = o[0]; p.x1 = x1[0]; p.z1 = z1[0]; p.z2t = z2t[0]; p.gran = gran;
  p.cnt = cnt; p.zero_words = zero_words; p.phases = 4;
  fused_phases<<<256, 512, 0, stream>>>(p);
  p.zero_words = 0; p.phases = 24;
  fused_phases<<<256, 512, 0, stream>>>(p);
  p.xin = x2; p.o = o[1]; p.x1 = x1[1]; p.z1 = z1[1]; p.z2t = z2t[1]; p.gran = gran + 16384;
  p.cnt = cnt + 4096; p.phases = 31;
  fused_phases<<<256, 512, 0, stream>>>(p);
  p.z2t_in = z2t[1]; p.z1_in = z1[1]; p.x1_in = x1[1]; p.h = h[1]; p.xout = (u16*)d_out;
  p.cnt = cnt + 8192; p.phases = 3;
  fused_phases<<<256, 512, 0, stream>>>(p);
}
```
